```python
import torch

def _softmax(x, axis=-1):
    return torch.softmax(x, dim=axis)
def _gelu(x):
    return torch.nn.functional.gelu(x, approximate="tanh")
def _silu(x):
    return torch.nn.functional.silu(x)
def _sigmoid(x):
    return torch.sigmoid(x)
def _layernorm(x, axis=-1, eps=1e-5):
    m = x.mean(dim=axis, keepdim=True)
    v = x.var(dim=axis, keepdim=True, unbiased=False)
    return (x - m) / torch.sqrt(v + eps)
def _scatter_add(tgt, idx, src, axis=0):
    return tgt.clone().index_add_(axis, idx, src)
def _segment_sum(src, seg, n):
    out = torch.zeros((n,) + src.shape[1:], dtype=src.dtype)
    return out.index_add_(0, seg, src)
def _pad(x, pw):
    flat = tuple(v for pair in reversed(pw) for v in pair)
    return torch.nn.functional.pad(x, flat)

kernel_name = 'concat_heads__mha_block_cached__gqa_block_cached__near_ridge'

def setup_inputs():

    _w = (lambda a: (lambda t: t.to(torch.bfloat16) if t.is_floating_point() else t)(torch.as_tensor(a)))
    def _A_setup():
        B, Sq, D = 8, 160, 576
        H, Sk, Dh = 32, 512, 128
        return {
            "x": _w((lambda *s: torch.randn(*s, dtype=torch.bfloat16) * 0.8)(B, Sq, D)),
            "k_cache": _w((lambda *s: torch.randn(*s, dtype=torch.bfloat16) * 0.8)(B, H, Sk, Dh)),
            "v_cache": _w((lambda *s: torch.randn(*s, dtype=torch.bfloat16) * 0.05)(B, H, Sk, Dh)),
            "wq": _w((lambda *s: torch.randn(*s, dtype=torch.bfloat16) * 0.8)(D, H * Dh) * (D ** -0.5)),
            "wo": _w((lambda *s: torch.randn(*s, dtype=torch.bfloat16) * 0.05)(H * Dh, D) * ((H * Dh) ** -0.5)),
        }
    _A_in = _A_setup()

    def _B_setup():
        B, Sq, D = 8, 160, 576
        Hq, Hkv, Sk, Dh = 32, 32, 512, 128
        assert Hq % Hkv == 0, (Hq, Hkv)
        return {
            "x": _w((lambda *s: torch.randn(*s, dtype=torch.bfloat16) * 0.8)(B, Sq, D)),
            "k_cache": _w((lambda *s: torch.randn(*s, dtype=torch.bfloat16) * 0.8)(B, Hkv, Sk, Dh)),
            "v_cache": _w((lambda *s: torch.randn(*s, dtype=torch.bfloat16) * 0.05)(B, Hkv, Sk, Dh)),
            "wq": _w((lambda *s: torch.randn(*s, dtype=torch.bfloat16) * 0.8)(D, Hq * Dh) * (D ** -0.5)),
            "wo": _w((lambda *s: torch.randn(*s, dtype=torch.bfloat16) * 0.05)(Hq * Dh, D) * ((Hq * Dh) ** -0.5)),
        }
    _B_in = _B_setup()

    return {
        "A_x": _A_in["x"],
        "A_k_cache": _A_in["k_cache"],
        "A_v_cache": _A_in["v_cache"],
        "A_wq": _A_in["wq"],
        "A_wo": _A_in["wo"],
        "B_x": _B_in["x"],
        "B_k_cache": _B_in["k_cache"],
        "B_v_cache": _B_in["v_cache"],
        "B_wq": _B_in["wq"],
        "B_wo": _B_in["wo"],
    }

def reference(A_x, A_k_cache, A_v_cache, A_wq, A_wo, B_x, B_k_cache, B_v_cache, B_wq, B_wo):
    def _A_ref(x, k_cache, v_cache, wq, wo):
        B, Sq, D = x.shape
        H, Dh = 32, 128
        q = (lambda x, *d: x.permute(*d))((x @ wq).reshape(B, Sq, H, Dh), 0, 2, 1, 3)
        scores = torch.einsum("bhqd,bhkd->bhqk", q, k_cache) * (Dh ** -0.5)
        p = _softmax(scores, axis=-1)
        o = torch.einsum("bhqk,bhkd->bhqd", p, v_cache)
        return (lambda x, *d: x.permute(*d))(o, 0, 2, 1, 3).reshape(B, Sq, H * Dh) @ wo

    def _B_ref(x, k_cache, v_cache, wq, wo):
        B, Sq, D = x.shape
        Hq, Dh = 32, 128
        Hkv = k_cache.shape[1]
        rep = Hq // Hkv
        q = (lambda x, *d: x.permute(*d))((x @ wq).reshape(B, Sq, Hq, Dh), 0, 2, 1, 3)
        k = (lambda x, n, axis: torch.repeat_interleave(x, n, dim=axis))(k_cache, rep, axis=1)
        v = (lambda x, n, axis: torch.repeat_interleave(x, n, dim=axis))(v_cache, rep, axis=1)
        scores = torch.einsum("bhqd,bhkd->bhqk", q, k) * (Dh ** -0.5)
        p = _softmax(scores, axis=-1)
        o = torch.einsum("bhqk,bhkd->bhqd", p, v)
        return (lambda x, *d: x.permute(*d))(o, 0, 2, 1, 3).reshape(B, Sq, Hq * Dh) @ wo

    a = _A_ref(A_x, A_k_cache, A_v_cache, A_wq, A_wo)
    b = _B_ref(B_x, B_k_cache, B_v_cache, B_wq, B_wo)
    return torch.concatenate([a, b], axis=-1)
```

```cpp
#include <hip/hip_runtime.h>
#include <hip/hip_bf16.h>

typedef short s16x4 __attribute__((ext_vector_type(4)));
typedef short s16x8 __attribute__((ext_vector_type(8)));
typedef __bf16 bf16x2 __attribute__((ext_vector_type(2)));
typedef __bf16 bf16x8 __attribute__((ext_vector_type(8)));
typedef float f32x2 __attribute__((ext_vector_type(2)));
typedef float f32x4 __attribute__((ext_vector_type(4)));
typedef unsigned u32x2 __attribute__((ext_vector_type(2)));
typedef unsigned u32x4 __attribute__((ext_vector_type(4)));
typedef __attribute__((address_space(3))) s16x4* lds_s16x4_ptr;
typedef __amdgpu_buffer_rsrc_t srd_t;

#define DI __device__ __forceinline__

constexpr int NB = 8, SQ = 160, DM = 576, NH = 32, SK = 512, DH = 128;
constexpr int HD = NH * DH;
constexpr float SCALE_LOG2E = 0.08838834764831845f * 1.4426950408889634f;
constexpr float RESCALE_THR = 8.0f;

#define KV_NT 1
#define QW_NT 0
#define O_NT 0
#define WO_NT 0

DI unsigned rfl(unsigned x) { return __builtin_amdgcn_readfirstlane(x); }
DI srd_t make_srd(const void* p, unsigned bytes) { return __builtin_amdgcn_make_buffer_rsrc((void*)p, (short)0, (int)bytes, 0x00020000); }

template <int NT>
DI void dma16(srd_t srd, unsigned voff, unsigned soff, unsigned ldsaddr) {
  if constexpr (NT)
    asm volatile("s_mov_b32 m0, %0\n\ts_nop 0\n\tbuffer_load_dwordx4 %1, %2, %3 offen nt lds" ::"s"(ldsaddr), "v"(voff), "s"(srd), "s"(soff) : "memory");
  else
    asm volatile("s_mov_b32 m0, %0\n\ts_nop 0\n\tbuffer_load_dwordx4 %1, %2, %3 offen lds" ::"s"(ldsaddr), "v"(voff), "s"(srd), "s"(soff) : "memory");
}
template <int N>
DI void wait_barrier() { asm volatile("s_waitcnt vmcnt(%0) lgkmcnt(0)\n\ts_barrier" ::"i"(N) : "memory"); }

DI unsigned cvtpk(float lo, float hi) {
  f32x2 v = {lo, hi};
  bf16x2 b = __builtin_convertvector(v, bf16x2);
  return __builtin_bit_cast(unsigned, b);
}
DI f32x4 mfma16(bf16x8 a, bf16x8 b, f32x4 c) { return __builtin_amdgcn_mfma_f32_16x16x32_bf16(a, b, c, 0, 0, 0); }
DI bf16x8 lds_read128(const char* lds, unsigned off) { return *reinterpret_cast<const bf16x8*>(lds + off); }
DI bf16x8 lds_read_tr2(const char* lds, unsigned off0, unsigned off1) {
  s16x4 a = __builtin_amdgcn_ds_read_tr16_b64_v4i16((lds_s16x4_ptr)(lds + off0));
  s16x4 b = __builtin_amdgcn_ds_read_tr16_b64_v4i16((lds_s16x4_ptr)(lds + off1));
  s16x8 r = __builtin_shufflevector(a, b, 0, 1, 2, 3, 4, 5, 6, 7);
  return __builtin_bit_cast(bf16x8, r);
}
DI float fmul_s(float a, float b) { float r; asm("v_mul_f32 %0, %1, %2" : "=v"(r) : "v"(a), "v"(b)); return r; }
DI float fsub_s(float a, float b) { float r; asm("v_sub_f32 %0, %1, %2" : "=v"(r) : "v"(a), "v"(b)); return r; }
DI void mfma_settle(f32x4& a) { asm volatile("s_nop 7\n\ts_nop 1" : "+v"(a)); }
DI float xmax16_32(float v) {
  unsigned u = __float_as_uint(v);
  auto a = __builtin_amdgcn_permlane16_swap(u, u, false, false);
  v = fmaxf(__uint_as_float(a[0]), __uint_as_float(a[1]));
  u = __float_as_uint(v);
  auto b = __builtin_amdgcn_permlane32_swap(u, u, false, false);
  return fmaxf(__uint_as_float(b[0]), __uint_as_float(b[1]));
}

constexpr unsigned PAIR = 32768, QST0 = 65536, QSTB = 26624, QST_X = 16384;
constexpr int K1_LDS = 163840 - 16384;

template <int NQ>
DI void attn_wave(char* lds, const int w, const int lane, const char* xb, const char* wqb, const char* kb0, const char* vb0,
                  char* ows, const int rowtile0, const int head0) {
  constexpr bool BIG = (NQ == 3);
  const int hh = (w >> 1) & 1, wi = w & 1, j4 = w & 3;
  const int g = lane >> 4, l15 = lane & 15;
  const int qt0 = BIG ? 3 * wi : 6 + 2 * wi;
  const unsigned ldsb = (unsigned)(size_t)lds;

  const srd_t srdK0 = make_srd(kb0, SK * DH * 2), srdK1 = make_srd(kb0 + SK * DH * 2, SK * DH * 2);
  const srd_t srdV0 = make_srd(vb0, SK * DH * 2), srdV1 = make_srd(vb0 + SK * DH * 2, SK * DH * 2);
  const unsigned krow = 4 * j4 + g;
  const unsigned vK = krow * 256 + 16 * (l15 ^ krow);
  const unsigned tr_row = (lane >> 2) & 7, tr_col = (lane >> 5) * 64 + 16 * ((lane & 3) ^ (2 * (tr_row >> 2)));
  const unsigned vV = tr_row * 256 + tr_col;
  auto issue_kv = [&](int step, int slot) {
    const unsigned so = step * 8192, lb = ldsb + slot * PAIR;
#pragma unroll
    for (int i = 0; i < 2; ++i) {
      dma16<KV_NT>(srdK0, vK, so + i * 4096, lb + (j4 + 4 * i) * 1024);
      dma16<KV_NT>(srdK1, vK, so + i * 4096, lb + 16384 + (j4 + 4 * i) * 1024);
    }
#pragma unroll
    for (int i = 0; i < 2; ++i) {
      dma16<KV_NT>(srdV0, vV, so + j4 * 2048 + i * 128, lb + 8192 + (2 * j4 + i) * 1024);
      dma16<KV_NT>(srdV1, vV, so + j4 * 2048 + i * 128, lb + 24576 + (2 * j4 + i) * 1024);
    }
  };

  f32x4 qa[8][NQ];
#pragma unroll
  for (int dt = 0; dt < 8; ++dt)
#pragma unroll
    for (int t = 0; t < NQ; ++t) qa[dt][t] = f32x4{0.f, 0.f, 0.f, 0.f};

  const srd_t srdX = make_srd(xb, SQ * DM * 2), srdW = make_srd(wqb, (DM - 1) * HD * 2 + 512);
  const unsigned vX = (lane >> 2) * (DM * 2) + 16 * ((lane & 3) ^ ((0u - (lane >> 4)) & 3));
  const unsigned vW = (tr_row + 8 * j4) * (HD * 2) + (lane >> 5) * 64 + 16 * ((lane & 3) ^ (2 * (j4 & 1)));
  const unsigned xt2 = rfl(j4 < 2 ? j4 + 8 : 9);
  const unsigned xso2 = rfl(xt2 * 16 * (DM * 2));
  auto issue_q = [&](int ks, int buf) {
    const unsigned lb = ldsb + QST0 + buf * QSTB;
#pragma unroll
    for (int pc = 0; pc < 4; ++pc) dma16<QW_NT>(srdW, vW, ks * 32 * (HD * 2) + pc * 128, lb + (j4 * 4 + pc) * 1024);
#pragma unroll
    for (int i = 0; i < 2; ++i) dma16<QW_NT>(srdX, vX, (j4 + 4 * i) * 16 * (DM * 2) + ks * 64, lb + QST_X + (j4 + 4 * i) * 1024);
    dma16<QW_NT>(srdX, vX, xso2 + ks * 64, lb + QST_X + xt2 * 1024);
  };
  constexpr int NKS = DM / 32;
  if constexpr (BIG) { issue_kv(0, 0); issue_kv(1, 1); }
  else { issue_q(0, 0); issue_q(1, 1); }

  const unsigned aW0 = g * 4096 + hh * 2048 + ((lane >> 2) & 3) * 64 + 16 * (((lane & 3) >> 1) ^ (2 * (g & 1))) + 8 * (lane & 1);
  const unsigned aW1 = aW0 ^ 32;
  const unsigned aX = QST_X + qt0 * 1024 + l15 * 64 + 16 * (g ^ ((0u - (l15 >> 2)) & 3));
  for (int ks = 0, buf = 0; ks < NKS; ++ks) {
    if (BIG) wait_barrier<63>();
    else if (ks + 1 < NKS) wait_barrier<7>();
    else wait_barrier<0>();
    if (!BIG && ks + 2 < NKS) issue_q(ks + 2, buf == 0 ? 2 : buf - 1);
    const char* st = lds + QST0 + buf * QSTB;
    bf16x8 xf[NQ];
#pragma unroll
    for (int t = 0; t < NQ; ++t) xf[t] = lds_read128(st, aX + t * 1024);
#pragma unroll
    for (int dt = 0; dt < 8; ++dt) {
      const unsigned a = ((dt & 1) ? aW1 : aW0) + (dt >> 1) * 512;
      bf16x8 wf = lds_read_tr2(st, a, a + 256);
#pragma unroll
      for (int t = 0; t < NQ; ++t) qa[dt][t] = mfma16(wf, xf[t], qa[dt][t]);
    }
    buf = buf == 2 ? 0 : buf + 1;
  }
  bf16x8 qf[NQ][4];
#pragma unroll
  for (int t = 0; t < NQ; ++t)
#pragma unroll
    for (int s = 0; s < 4; ++s) {
      mfma_settle(qa[2 * s][t]);
      mfma_settle(qa[2 * s + 1][t]);
      const float c = SCALE_LOG2E;
      unsigned r0a = cvtpk(fmul_s(qa[2 * s][t][0], c), fmul_s(qa[2 * s][t][1], c)), r0b = cvtpk(fmul_s(qa[2 * s][t][2], c), fmul_s(qa[2 * s][t][3], c));
      unsigned r1a = cvtpk(fmul_s(qa[2 * s + 1][t][0], c), fmul_s(qa[2 * s + 1][t][1], c)), r1b = cvtpk(fmul_s(qa[2 * s + 1][t][2], c), fmul_s(qa[2 * s + 1][t][3], c));
      auto a32 = __builtin_amdgcn_permlane32_swap(r0a, r1a, false, false);
      auto b32 = __builtin_amdgcn_permlane32_swap(r0b, r1b, false, false);
      auto a16 = __builtin_amdgcn_permlane16_swap(a32[0], a32[1], false, false);
      auto b16 = __builtin_amdgcn_permlane16_swap(b32[0], b32[1], false, false);
      u32x4 f = {a16[0], b16[0], a16[1], b16[1]};
      qf[t][s] = __builtin_bit_cast(bf16x8, f);
    }
  wait_barrier<63>();
  if constexpr (!BIG) issue_kv(2, 2);

  f32x4 oa[8][NQ], la[NQ], nm[NQ];
#pragma unroll
  for (int t = 0; t < NQ; ++t) {
    la[t] = f32x4{0.f, 0.f, 0.f, 0.f};
    nm[t] = f32x4{0.f, 0.f, 0.f, 0.f};
#pragma unroll
    for (int dt = 0; dt < 8; ++dt) oa[dt][t] = f32x4{0.f, 0.f, 0.f, 0.f};
  }
  const u32x4 onesu = {0x3F803F80u, 0x3F803F80u, 0x3F803F80u, 0x3F803F80u};
  const bf16x8 ones = __builtin_bit_cast(bf16x8, onesu);
  unsigned aK[4];
#pragma unroll
  for (int s = 0; s < 4; ++s) aK[s] = hh * 16384 + l15 * 256 + 16 * ((4 * s + g) ^ l15);
  const unsigned aV0 = hh * 16384 + 8192 + (g >> 1) * 2048 + (4 * (g & 1) + ((lane >> 2) & 3)) * 64 + 16 * (((lane & 3) >> 1) ^ (2 * (g & 1))) + 8 * (lane & 1);
  const unsigned aV1 = aV0 ^ 32;

  constexpr int NST = SK / 32;
  for (int st = 0; st < NST; ++st) {
    if (BIG) { if (st == 0) wait_barrier<8>(); else wait_barrier<0>(); }
    else { if (st + 2 < NST) wait_barrier<16>(); else if (st + 1 < NST) wait_barrier<8>(); else wait_barrier<0>(); }
    if (!BIG && st + 3 < NST) issue_kv(st + 3, (st + 3) & 3);
    const char* sl = lds + (st & 3) * PAIR;

    f32x4 sa[2][NQ];
#pragma unroll
    for (int kt = 0; kt < 2; ++kt) {
#pragma unroll
      for (int t = 0; t < NQ; ++t) sa[kt][t] = nm[t];
#pragma unroll
      for (int s = 0; s < 4; ++s) {
        bf16x8 kf = lds_read128(sl, aK[s] + kt * 4096);
#pragma unroll
        for (int t = 0; t < NQ; ++t) sa[kt][t] = mfma16(kf, qf[t][s], sa[kt][t]);
      }
    }
    float pm = -3.0e38f;
#pragma unroll
    for (int t = 0; t < NQ; ++t)
#pragma unroll
      for (int kt = 0; kt < 2; ++kt)
#pragma unroll
        for (int r = 0; r < 4; ++r) pm = fmaxf(pm, sa[kt][t][r]);
    if (st == 0 || __any(pm > RESCALE_THR)) {
#pragma unroll
      for (int t = 0; t < NQ; ++t) {
        float rm = fmaxf(fmaxf(fmaxf(sa[0][t][0], sa[0][t][1]), fmaxf(sa[0][t][2], sa[0][t][3])),
                         fmaxf(fmaxf(sa[1][t][0], sa[1][t][1]), fmaxf(sa[1][t][2], sa[1][t][3])));
        rm = xmax16_32(rm);
        const float dl = (st == 0) ? rm : fmaxf(rm, 0.f);
        const float f = (st == 0) ? 1.f : __builtin_amdgcn_exp2f(-dl);
        mfma_settle(sa[0][t]);
        mfma_settle(sa[1][t]);
        mfma_settle(la[t]);
#pragma unroll
        for (int r = 0; r < 4; ++r) {
          nm[t][r] = fsub_s(nm[t][r], dl);
          sa[0][t][r] = fsub_s(sa[0][t][r], dl);
          sa[1][t][r] = fsub_s(sa[1][t][r], dl);
          la[t][r] = fmul_s(la[t][r], f);
        }
#pragma unroll
        for (int dt = 0; dt < 8; ++dt) {
          mfma_settle(oa[dt][t]);
#pragma unroll
          for (int r = 0; r < 4; ++r) oa[dt][t][r] = fmul_s(oa[dt][t][r], f);
        }
      }
      asm volatile("s_nop 1" ::: "memory");
    }
    bf16x8 pf[NQ];
#pragma unroll
    for (int t = 0; t < NQ; ++t) {
      u32x4 u;
      u[0] = cvtpk(__builtin_amdgcn_exp2f(sa[0][t][0]), __builtin_amdgcn_exp2f(sa[0][t][1]));
      u[1] = cvtpk(__builtin_amdgcn_exp2f(sa[0][t][2]), __builtin_amdgcn_exp2f(sa[0][t][3]));
      u[2] = cvtpk(__builtin_amdgcn_exp2f(sa[1][t][0]), __builtin_amdgcn_exp2f(sa[1][t][1]));
      u[3] = cvtpk(__builtin_amdgcn_exp2f(sa[1][t][2]), __builtin_amdgcn_exp2f(sa[1][t][3]));
      pf[t] = __builtin_bit_cast(bf16x8, u);
      la[t] = mfma16(ones, pf[t], la[t]);
    }
#pragma unroll
    for (int dt = 0; dt < 8; ++dt) {
      const unsigned a = ((dt & 1) ? aV1 : aV0) + (dt >> 1) * 512;
      bf16x8 vf = lds_read_tr2(sl, a, a + 4096);
#pragma unroll
      for (int t = 0; t < NQ; ++t) oa[dt][t] = mfma16(vf, pf[t], oa[dt][t]);
    }
  }

  const int head = head0 + hh;
#pragma unroll
  for (int t = 0; t < NQ; ++t) {
    const float inv = 1.0f / la[t][0];
    char* dst = ows + ((size_t)(rowtile0 + qt0 + t) * 128 + head * 4) * 1024 + lane * 16;
#pragma unroll
    for (int u = 0; u < 4; ++u) {
      u32x4 o;
      mfma_settle(oa[2 * u][t]);
      mfma_settle(oa[2 * u + 1][t]);
      o[0] = cvtpk(fmul_s(oa[2 * u][t][0], inv), fmul_s(oa[2 * u][t][1], inv));
      o[1] = cvtpk(fmul_s(oa[2 * u][t][2], inv), fmul_s(oa[2 * u][t][3], inv));
      o[2] = cvtpk(fmul_s(oa[2 * u + 1][t][0], inv), fmul_s(oa[2 * u + 1][t][1], inv));
      o[3] = cvtpk(fmul_s(oa[2 * u + 1][t][2], inv), fmul_s(oa[2 * u + 1][t][3], inv));
      *reinterpret_cast<u32x4*>(dst + u * 1024) = o;
    }
  }
}

__global__ __launch_bounds__(512, 2) void attn_kernel(const char* __restrict__ xA, const char* __restrict__ kA, const char* __restrict__ vA,
                                                       const char* __restrict__ wqA, const char* __restrict__ xB, const char* __restrict__ kB,
                                                       const char* __restrict__ vB, const char* __restrict__ wqB, char* __restrict__ ows) {
  extern __shared__ __attribute__((aligned(16))) char lds[];
  const int tid = threadIdx.x, lane = tid & 63;
  const int w = (int)rfl(tid >> 6);
  const int xcd = blockIdx.x & 7, idx = blockIdx.x >> 3;
  const int branch = xcd >> 2, b = idx >> 2, head0 = 8 * (xcd & 3) + 2 * (idx & 3);
  const char* x = branch ? xB : xA;
  const char* k = branch ? kB : kA;
  const char* v = branch ? vB : vA;
  const char* wq = branch ? wqB : wqA;
  const char* xb = x + (size_t)b * SQ * DM * 2;
  const char* wqb = wq + head0 * DH * 2;
  const size_t kvo = (size_t)(b * NH + head0) * SK * DH * 2;
  const int rowtile0 = (branch * NB + b) * 10;
  if (w < 4) attn_wave<3>(lds, w, lane, xb, wqb, k + kvo, v + kvo, ows, rowtile0, head0);
  else attn_wave<2>(lds, w, lane, xb, wqb, k + kvo, v + kvo, ows, rowtile0, head0);
}

constexpr unsigned OSTB = 40960, OST_W = 16384;
constexpr int K2_LDS = 163840;

__global__ __launch_bounds__(512, 2) void oproj_kernel(const char* __restrict__ ows, const char* __restrict__ woA, const char* __restrict__ woB,
                                                        char* __restrict__ out) {
  extern __shared__ __attribute__((aligned(16))) char lds[];
  const int tid = threadIdx.x, lane = tid & 63;
  const int w = (int)rfl(tid >> 6);
  const int xcd = blockIdx.x & 7, idx = blockIdx.x >> 3;
  const int branch = xcd >> 2, rb = 5 * (xcd & 3) + idx / 6, cb = idx % 6;
  const int g = lane >> 4, l15 = lane & 15;
  const unsigned ldsb = (unsigned)(size_t)lds;
  const char* wo = (branch ? woB : woA) + cb * 96 * 2;
  const srd_t srdO = make_srd(ows + (size_t)(branch * 80 + rb * 4) * 128 * 1024, 4 * 128 * 1024);
  const srd_t srdW = make_srd(wo, (HD - 1) * DM * 2 + 192);

  unsigned vW[3];
#pragma unroll
  for (int i = 0; i < 3; ++i) {
    const unsigned sidx = 2 * i + (lane >> 5), kb = sidx / 3, sc = sidx % 3;
    const unsigned r = (lane >> 2) & 7;
    vW[i] = (8 * kb + r) * (DM * 2) + sc * 64 + 16 * ((lane & 3) ^ (2 * (r >> 2)));
  }
  const unsigned vO = lane * 16;
  auto issue = [&](int t, int buf) {
    const unsigned lb = ldsb + buf * OSTB;
#pragma unroll
    for (int i = 0; i < 2; ++i)
      dma16<O_NT>(srdO, vO, ((w >> 1) * 128 + 4 * t + 2 * (w & 1) + i) * 1024, lb + (2 * w + i) * 1024);
#pragma unroll
    for (int i = 0; i < 3; ++i) dma16<WO_NT>(srdW, vW[i], (t * 128 + 16 * w) * (DM * 2), lb + OST_W + (3 * w + i) * 1024);
  };
  constexpr int NT = HD / 128;
  issue(0, 0); issue(1, 1); issue(2, 2);

  const int kh = w >> 2, wm = (w >> 1) & 1, wn = w & 1;
  const unsigned aWb = OST_W + (g >> 1) * 1536 + (4 * (g & 1) + ((lane >> 2) & 3)) * 64 + 16 * (((lane & 3) >> 1) ^ (2 * (g & 1))) + 8 * (lane & 1) + kh * 2 * 6144;
  unsigned aW[3];
#pragma unroll
  for (int n = 0; n < 3; ++n) { const unsigned nt = 3 * wn + n; aW[n] = (aWb ^ ((nt & 1) * 32)) + (nt >> 1) * 512; }
  const unsigned aO = (2 * wm * 4 + 2 * kh) * 1024 + lane * 16;
  f32x4 acc[3][2];
#pragma unroll
  for (int n = 0; n < 3; ++n)
#pragma unroll
    for (int m = 0; m < 2; ++m) acc[n][m] = f32x4{0.f, 0.f, 0.f, 0.f};

  for (int t = 0; t < NT; ++t) {
    if (t + 2 < NT) wait_barrier<10>(); else if (t + 1 < NT) wait_barrier<5>(); else wait_barrier<0>();
    if (t + 3 < NT) issue(t + 3, (t + 3) & 3);
    const char* st = lds + (t & 3) * OSTB;
#pragma unroll
    for (int kk = 0; kk < 2; ++kk) {
      bf16x8 of[2];
#pragma unroll
      for (int m = 0; m < 2; ++m) of[m] = lds_read128(st, aO + (m * 4 + kk) * 1024);
#pragma unroll
      for (int n = 0; n < 3; ++n) {
        bf16x8 wf = lds_read_tr2(st, aW[n] + kk * 6144, aW[n] + kk * 6144 + 3072);
#pragma unroll
        for (int m = 0; m < 2; ++m) acc[n][m] = mfma16(wf, of[m], acc[n][m]);
      }
    }
  }
  wait_barrier<0>();
  f32x4* red = reinterpret_cast<f32x4*>(lds) + (w & 3) * 6 * 64 + lane;
  if (kh == 1) {
#pragma unroll
    for (int n = 0; n < 3; ++n)
#pragma unroll
      for (int m = 0; m < 2; ++m) red[(n * 2 + m) * 64] = acc[n][m];
  }
  wait_barrier<0>();
  if (kh == 0) {
#pragma unroll
    for (int m = 0; m < 2; ++m) {
      const int row = rb * 64 + (2 * wm + m) * 16 + l15;
      char* dst = out + (size_t)row * (2 * DM * 2) + (branch * DM + cb * 96 + wn * 48 + 4 * g) * 2;
#pragma unroll
      for (int n = 0; n < 3; ++n) {
        const f32x4 a = acc[n][m] + red[(n * 2 + m) * 64];
        u32x2 o = {cvtpk(a[0], a[1]), cvtpk(a[2], a[3])};
        *reinterpret_cast<u32x2*>(dst + n * 32) = o;
      }
    }
  }
}

extern "C" void kernel_launch(void* const* d_in, const int* in_sizes, int n_in, void* d_out, int out_size, void* d_ws, size_t ws_size,
                              hipStream_t stream) {
  (void)in_sizes; (void)n_in; (void)out_size; (void)ws_size;
  (void)hipFuncSetAttribute((const void*)attn_kernel, hipFuncAttributeMaxDynamicSharedMemorySize, K1_LDS);
  (void)hipFuncSetAttribute((const void*)oproj_kernel, hipFuncAttributeMaxDynamicSharedMemorySize, K2_LDS);
  char* ows = (char*)d_ws;
  attn_kernel<<<256, 512, K1_LDS, stream>>>((const char*)d_in[0], (const char*)d_in[1], (const char*)d_in[2], (const char*)d_in[3],
                                            (const char*)d_in[5], (const char*)d_in[6], (const char*)d_in[7], (const char*)d_in[8], ows);
  oproj_kernel<<<240, 512, K2_LDS, stream>>>(ows, (const char*)d_in[4], (const char*)d_in[9], (char*)d_out);
}
```

```cpp
#include <hip/hip_runtime.h>
#include <hip/hip_bf16.h>

typedef short s16x4 __attribute__((ext_vector_type(4)));
typedef short s16x8 __attribute__((ext_vector_type(8)));
typedef __bf16 bf16x2 __attribute__((ext_vector_type(2)));
typedef __bf16 bf16x8 __attribute__((ext_vector_type(8)));
typedef float f32x2 __attribute__((ext_vector_type(2)));
typedef float f32x4 __attribute__((ext_vector_type(4)));
typedef unsigned u32x2 __attribute__((ext_vector_type(2)));
typedef unsigned u32x4 __attribute__((ext_vector_type(4)));
typedef __attribute__((address_space(3))) s16x4* lds_s16x4_ptr;
typedef __attribute__((address_space(1))) unsigned long long gu64;
typedef __amdgpu_buffer_rsrc_t srd_t;

#define DI __device__ __forceinline__
#define SB() __builtin_amdgcn_sched_barrier(0)

constexpr int NB = 8, SQ = 160, DM = 576, NH = 32, SK = 512, DH = 128;
constexpr int HD = NH * DH;
constexpr float SCALE_LOG2E = 0.08838834764831845f * 1.4426950408889634f;
constexpr float RESCALE_THR = 8.0f;
constexpr unsigned long long TAG_STEP = 0x9E3779B97F4A7C15ull;
constexpr unsigned SPIN_CAP = 1u << 20;

constexpr size_t WS_O = 0;
constexpr size_t WS_PART = (size_t)160 * 128 * 1024;
constexpr size_t WS_FLAGS = WS_PART + (size_t)60 * 16 * 12288;
constexpr size_t WS_EPOCH = WS_FLAGS + 8192;

#define KV_NT 1
#define KPD_BIG 2
#define VPD_BIG 2
#define VPD_SMALL 4
#define P2_BURST 1
#define TOUCH_Q 0
#define TOUCH_NT 0
#define Q_NT 0

DI unsigned rfl(unsigned x) { return __builtin_amdgcn_readfirstlane(x); }
DI srd_t make_srd(const void* p, unsigned bytes) { return __builtin_amdgcn_make_buffer_rsrc((void*)p, (short)0, (int)bytes, 0x00020000); }

template <int NT>
DI void dma16(srd_t srd, unsigned voff, unsigned soff, unsigned ldsaddr) {
  if constexpr (NT)
    asm volatile("s_mov_b32 m0, %0\n\ts_nop 0\n\tbuffer_load_dwordx4 %1, %2, %3 offen nt lds" ::"s"(ldsaddr), "v"(voff), "s"(srd), "s"(soff) : "memory");
  else
    asm volatile("s_mov_b32 m0, %0\n\ts_nop 0\n\tbuffer_load_dwordx4 %1, %2, %3 offen lds" ::"s"(ldsaddr), "v"(voff), "s"(srd), "s"(soff) : "memory");
}
template <int POL>
DI void touch(const char* p, unsigned ldsaddr) {
  if constexpr (POL == 1) asm volatile("s_mov_b32 m0, %0\n\ts_nop 0\n\tglobal_load_lds_dword %1, off nt" ::"s"(ldsaddr), "v"(p) : "memory");
  else if constexpr (POL == 2) asm volatile("s_mov_b32 m0, %0\n\ts_nop 0\n\tglobal_load_lds_dword %1, off sc1" ::"s"(ldsaddr), "v"(p) : "memory");
  else asm volatile("s_mov_b32 m0, %0\n\ts_nop 0\n\tglobal_load_lds_dword %1, off" ::"s"(ldsaddr), "v"(p) : "memory");
}
template <int N>
DI void wait_barrier() { asm volatile("s_waitcnt vmcnt(%0) lgkmcnt(0)\n\ts_barrier" ::"i"(N) : "memory"); }

DI unsigned cvtpk(float lo, float hi) {
  f32x2 v = {lo, hi};
  bf16x2 b = __builtin_convertvector(v, bf16x2);
  return __builtin_bit_cast(unsigned, b);
}
DI void mfma_acc(f32x4& c, bf16x8 a, bf16x8 b) { asm volatile("v_mfma_f32_16x16x32_bf16 %0, %1, %2, %0" : "+v"(c) : "v"(a), "v"(b)); }
DI void mfma_new(f32x4& d, bf16x8 a, bf16x8 b, f32x4 c) { asm volatile("v_mfma_f32_16x16x32_bf16 %0, %1, %2, %3" : "=&v"(d) : "v"(a), "v"(b), "v"(c)); }
DI void mfma_zero(f32x4& d, bf16x8 a, bf16x8 b) { asm volatile("v_mfma_f32_16x16x32_bf16 %0, %1, %2, 0" : "=&v"(d) : "v"(a), "v"(b)); }
template <bool ZERO>
DI void mfma_az(f32x4& c, bf16x8 a, bf16x8 b) { if constexpr (ZERO) mfma_zero(c, a, b); else mfma_acc(c, a, b); }
DI void mfma_settle() { asm volatile("s_nop 7\n\ts_nop 1" ::: "memory"); __builtin_amdgcn_sched_barrier(0); }
DI bf16x8 lds_read128(const char* lds, unsigned off) { return *reinterpret_cast<const bf16x8*>(lds + off); }
DI bf16x8 lds_read_tr2(const char* lds, unsigned off0, unsigned off1) {
  s16x4 a = __builtin_amdgcn_ds_read_tr16_b64_v4i16((lds_s16x4_ptr)(lds + off0));
  s16x4 b = __builtin_amdgcn_ds_read_tr16_b64_v4i16((lds_s16x4_ptr)(lds + off1));
  s16x8 r = __builtin_shufflevector(a, b, 0, 1, 2, 3, 4, 5, 6, 7);
  return __builtin_bit_cast(bf16x8, r);
}
DI f32x2 splat2(float v) { f32x2 p = {v, v}; asm volatile("" : "+v"(p)); return p; }
DI f32x4 mul4(f32x4 a, f32x2 p) {
  f32x2 lo = f32x2{a[0], a[1]} * p, hi = f32x2{a[2], a[3]} * p;
  return f32x4{lo[0], lo[1], hi[0], hi[1]};
}
DI f32x4 sub4(f32x4 a, f32x2 p) {
  f32x2 lo = f32x2{a[0], a[1]} - p, hi = f32x2{a[2], a[3]} - p;
  return f32x4{lo[0], lo[1], hi[0], hi[1]};
}
DI void mfma_move(f32x4& dst, f32x4 src, bf16x8 zero) { asm volatile("s_nop 1\n\tv_mfma_f32_16x16x32_bf16 %0, %1, %1, %2" : "+v"(dst) : "v"(zero), "v"(src)); }
DI bf16x8 zero_frag() { u32x4 z = {0u, 0u, 0u, 0u}; asm volatile("" : "+v"(z)); return __builtin_bit_cast(bf16x8, z); }
DI float max3_s(float a, float b, float c) { float r; asm volatile("v_max3_f32 %0, %1, %2, %3" : "=v"(r) : "v"(a), "v"(b), "v"(c)); return r; }
DI float xmax16_32(float v) {
  unsigned u = __float_as_uint(v);
  auto a = __builtin_amdgcn_permlane16_swap(u, u, false, false);
  v = fmaxf(__uint_as_float(a[0]), __uint_as_float(a[1]));
  u = __float_as_uint(v);
  auto b = __builtin_amdgcn_permlane32_swap(u, u, false, false);
  return fmaxf(__uint_as_float(b[0]), __uint_as_float(b[1]));
}
DI unsigned lane_id() { unsigned l; asm volatile("v_mbcnt_lo_u32_b32 %0, -1, 0\n\tv_mbcnt_hi_u32_b32 %0, -1, %0" : "=v"(l)); return l; }
template <int I, int N, class F>
DI void static_for(F&& f) {
  if constexpr (I < N) { f(std::integral_constant<int, I>{}); static_for<I + 1, N>(f); }
}

constexpr unsigned KRING = 0, VRING = 65536, SLOT = 16384, QST_X = 16384;
constexpr unsigned qstage(int i) { return i == 0 ? 32768u : (i == 1 ? 98304u : 124928u); }
constexpr unsigned FAC_LDS = 151552, TOUCH_LDS = 157696;
constexpr int LDS_BYTES = 163840;

template <int NQ>
DI void attn_wave(char* lds, const int w, const int lane, const char* xb, const char* wqb, const char* kb0, const char* vb0,
                  char* ows, const int rowtile0, const int head0, const char* wq_touch, const char* x_touch) {
  constexpr bool BIG = (NQ == 3);
  const int hh = (w >> 1) & 1, wi = w & 1, j4 = w & 3;
  const int g = lane >> 4, l15 = lane & 15;
  const int qt0 = BIG ? 3 * wi : 6 + 2 * wi;
  const unsigned ldsb = (unsigned)(size_t)lds;

  const srd_t srdK0 = make_srd(kb0, SK * DH * 2), srdK1 = make_srd(kb0 + SK * DH * 2, SK * DH * 2);
  const srd_t srdV0 = make_srd(vb0, SK * DH * 2), srdV1 = make_srd(vb0 + SK * DH * 2, SK * DH * 2);
  const unsigned krow = 4 * j4 + g;
  const unsigned vK = krow * 256 + 16 * (l15 ^ krow);
  const unsigned tr_row = (lane >> 2) & 7;
  const unsigned vV = tr_row * 256 + (lane >> 5) * 64 + 16 * ((lane & 3) ^ (2 * (tr_row >> 2)));
  auto dma_k = [&](int step, int slot, int i, int head) {
    dma16<KV_NT>(head ? srdK1 : srdK0, vK, step * 8192 + i * 4096, ldsb + KRING + slot * SLOT + head * 8192 + (j4 + 4 * i) * 1024);
  };
  auto dma_v = [&](int step, int slot, int i, int head) {
    dma16<KV_NT>(head ? srdV1 : srdV0, vV, step * 8192 + j4 * 2048 + i * 128, ldsb + VRING + slot * SLOT + head * 8192 + (2 * j4 + i) * 1024);
  };
  auto issue_k = [&](int step) { dma_k(step, step & 3, 0, 0); dma_k(step, step & 3, 0, 1); dma_k(step, step & 3, 1, 0); dma_k(step, step & 3, 1, 1); };
  auto issue_v = [&](int step) { dma_v(step, step & 3, 0, 0); dma_v(step, step & 3, 0, 1); dma_v(step, step & 3, 1, 0); dma_v(step, step & 3, 1, 1); };

  const srd_t srdX = make_srd(xb, SQ * DM * 2), srdW = make_srd(wqb, (DM - 1) * HD * 2 + 512);
  const unsigned vX = (lane >> 2) * (DM * 2) + 16 * ((lane & 3) ^ ((0u - (lane >> 4)) & 3));
  const unsigned vWp = (tr_row + 8 * j4) * (HD * 2) + (lane >> 5) * 64 + 16 * ((lane & 3) ^ (2 * (j4 & 1)));
  const unsigned kbw = w >> 1;
  const unsigned vW = (tr_row + 8 * kbw) * (HD * 2) + (lane >> 5) * 64 + 16 * ((lane & 3) ^ (2 * (kbw & 1))) + (w & 1) * 256;
  const unsigned xt2 = rfl(j4 < 2 ? j4 + 8 : 9);
  const unsigned xso2 = rfl(xt2 * 16 * (DM * 2));
  auto dma_q0 = [&](int ks, int i) {
    const unsigned lb = ldsb + qstage(ks % 3);
    if (i < 4) dma16<Q_NT>(srdW, vWp, ks * 32 * (HD * 2) + i * 128, lb + (j4 * 4 + i) * 1024);
    else if (i < 6) dma16<Q_NT>(srdX, vX, (j4 + 4 * (i - 4)) * 16 * (DM * 2) + ks * 64, lb + QST_X + (j4 + 4 * (i - 4)) * 1024);
    else dma16<Q_NT>(srdX, vX, xso2 + ks * 64, lb + QST_X + xt2 * 1024);
  };
  auto dma_q = [&](int ks, int buf, int i) {
    const unsigned lb = ldsb + qstage(buf);
    if (i < 2) dma16<Q_NT>(srdW, vW, ks * 32 * (HD * 2) + i * 128, lb + (kbw * 4 + 2 * (w & 1) + i) * 1024);
    else if (i == 2) dma16<Q_NT>(srdX, vX, w * 16 * (DM * 2) + ks * 64, lb + QST_X + w * 1024);
    else if (!BIG) dma16<Q_NT>(srdX, vX, xso2 + ks * 64, lb + QST_X + xt2 * 1024);
  };
  constexpr int NKS = DM / 32;
  if constexpr (!BIG) {
    static_for<0, 7>([&](auto i) { dma_q0(0, i); });
    static_for<0, 7>([&](auto i) { dma_q0(1, i); });
  } else if constexpr (TOUCH_Q) {
    const unsigned l0 = j4 * 72 + lane, l1 = j4 * 72 + 64 + (lane & 7), sc = ldsb + TOUCH_LDS;
    touch<TOUCH_NT>(wq_touch + (l0 >> 4) * (HD * 2) + (l0 & 15) * 128, sc);
    touch<TOUCH_NT>(x_touch + (j4 * 90 + lane) * 128, sc);
    touch<TOUCH_NT>(lane < 8 ? wq_touch + (l1 >> 4) * (HD * 2) + (l1 & 15) * 128 : x_touch + (j4 * 90 + 64 + (lane < 34 ? lane - 8 : 0)) * 128, sc);
  }

  auto prefetch_kv = [&](int tile) {
    if constexpr (BIG) { dma_k(tile, tile, 0, 0); dma_k(tile, tile, 0, 1); dma_k(tile, tile, 1, 0); dma_k(tile, tile, 1, 1); }
    else { dma_v(tile, tile, 0, 0); dma_v(tile, tile, 0, 1); dma_v(tile, tile, 1, 0); dma_v(tile, tile, 1, 1); }
  };
  f32x4 qa[8][NQ];
  {
    const unsigned aW0 = g * 4096 + hh * 2048 + ((lane >> 2) & 3) * 64 + 16 * (((lane & 3) >> 1) ^ (2 * (g & 1))) + 8 * (lane & 1);
    const unsigned aW1 = aW0 ^ 32;
    const unsigned aX = QST_X + qt0 * 1024 + l15 * 64 + 16 * (g ^ ((0u - (l15 >> 2)) & 3));
    bf16x8 xo[NQ], wd[3];
#pragma unroll
    for (int i = 0; i < 3; ++i) wd[i] = zero_frag();
#pragma unroll
    for (int t = 0; t < NQ; ++t) xo[t] = zero_frag();
#pragma unroll
    for (int dt = 0; dt < 8; ++dt)
#pragma unroll
      for (int t = 0; t < NQ; ++t) { qa[dt][t] = f32x4{0.f, 0.f, 0.f, 0.f}; asm volatile("" : "+v"(qa[dt][t])); }
    auto qstep = [&](const int ks, auto bufc) {
      constexpr int BUF = bufc;
      if constexpr (BIG) { if (ks < 2) wait_barrier<63>(); else if (ks + 1 == NKS) wait_barrier<4>(); else wait_barrier<3>(); }
      else { if (ks == 0) wait_barrier<7>(); else wait_barrier<4>(); }
      const char* st = lds + qstage(BUF);
      auto rdw = [&](int dt) { const unsigned a = ((dt & 1) ? aW1 : aW0) + (dt >> 1) * 512; return lds_read_tr2(st, a, a + 256); };
      bf16x8 xn[NQ], wf[8];
      const bool more = ks + 2 < NKS;
      if constexpr (!BIG) {
        static_for<0, NQ>([&](auto i) { xn[i] = lds_read128(st, aX + i * 1024); });
        static_for<0, 4>([&](auto i) { wf[i] = rdw(i); });
        SB();
        if (more) { static_for<0, 4>([&](auto i) { dma_q(ks + 2, (BUF + 2) % 3, i); }); }
        else prefetch_kv(ks - (NKS - 2));
        SB();
      }
      static_for<0, 3>([&](auto ic) {
        constexpr int i = ic;
        if constexpr (BIG) { xn[i] = lds_read128(st, aX + i * 1024); wf[i] = rdw(i); }
#pragma unroll
        for (int t = 0; t < NQ; ++t) mfma_acc(qa[5 + i][t], wd[i], xo[t]);
        SB();
      });
      constexpr int QPD = BIG ? 3 : 4;
      static_for<0, 5>([&](auto dtc) {
        constexpr int dt = dtc;
        if constexpr (dt + QPD < 8) wf[dt + QPD] = rdw(dt + QPD);
#pragma unroll
        for (int t = 0; t < NQ; ++t) mfma_acc(qa[dt][t], wf[dt], xn[t]);
        SB();
      });
      if constexpr (BIG) {
        if (more) { static_for<0, 3>([&](auto i) { dma_q(ks + 2, (BUF + 2) % 3, i); }); }
        else prefetch_kv(ks - (NKS - 2));
        SB();
      }
#pragma unroll
      for (int i = 0; i < 3; ++i) wd[i] = wf[5 + i];
#pragma unroll
      for (int t = 0; t < NQ; ++t) xo[t] = xn[t];
    };
    for (int ks = 0; ks < NKS; ks += 3) {
      qstep(ks, std::integral_constant<int, 0>{});
      qstep(ks + 1, std::integral_constant<int, 1>{});
      qstep(ks + 2, std::integral_constant<int, 2>{});
    }
#pragma unroll
    for (int i = 0; i < 3; ++i)
#pragma unroll
      for (int t = 0; t < NQ; ++t) mfma_acc(qa[5 + i][t], wd[i], xo[t]);
  }
  mfma_settle();
  wait_barrier<63>();
  bf16x8 qf[NQ][4];
  const f32x2 cc = splat2(SCALE_LOG2E);
#pragma unroll
  for (int t = 0; t < NQ; ++t)
#pragma unroll
    for (int s = 0; s < 4; ++s) {
      const f32x4 q0 = mul4(qa[2 * s][t], cc), q1 = mul4(qa[2 * s + 1][t], cc);
      unsigned r0a = cvtpk(q0[0], q0[1]), r0b = cvtpk(q0[2], q0[3]), r1a = cvtpk(q1[0], q1[1]), r1b = cvtpk(q1[2], q1[3]);
      auto a32 = __builtin_amdgcn_permlane32_swap(r0a, r1a, false, false);
      auto b32 = __builtin_amdgcn_permlane32_swap(r0b, r1b, false, false);
      auto a16 = __builtin_amdgcn_permlane16_swap(a32[0], a32[1], false, false);
      auto b16 = __builtin_amdgcn_permlane16_swap(b32[0], b32[1], false, false);
      u32x4 f = {a16[0], b16[0], a16[1], b16[1]};
      qf[t][s] = __builtin_bit_cast(bf16x8, f);
    }
  if constexpr (!BIG) issue_k(2);

  f32x4 oa[8][NQ], la[NQ], nm[NQ];
#pragma unroll
  for (int t = 0; t < NQ; ++t) {
    la[t] = f32x4{0.f, 0.f, 0.f, 0.f};
    asm volatile("" : "+v"(la[t]));
#pragma unroll
    for (int dt = 0; dt < 8; ++dt) { oa[dt][t] = f32x4{0.f, 0.f, 0.f, 0.f}; asm volatile("" : "+v"(oa[dt][t])); }
  }
  const u32x4 onesu = {0x3F803F80u, 0x3F803F80u, 0x3F803F80u, 0x3F803F80u};
  bf16x8 ones = __builtin_bit_cast(bf16x8, onesu);
  asm volatile("" : "+v"(ones));
  unsigned aK[4];
#pragma unroll
  for (int s = 0; s < 4; ++s) aK[s] = KRING + hh * 8192 + l15 * 256 + 16 * ((4 * s + g) ^ l15);
  const unsigned aV0 = VRING + hh * 8192 + (g >> 1) * 2048 + (4 * (g & 1) + ((lane >> 2) & 3)) * 64 + 16 * (((lane & 3) >> 1) ^ (2 * (g & 1))) + 8 * (lane & 1);
  const unsigned aV1 = aV0 ^ 32;
  auto rdv = [&](const char* vs, int dt) { const unsigned a = ((dt & 1) ? aV1 : aV0) + (dt >> 1) * 512; return lds_read_tr2(vs, a, a + 4096); };

  bf16x8 pf[NQ], vp0, vp1;
  auto facs = [&]() { return reinterpret_cast<float*>(lds + FAC_LDS) + w * 192 + lane_id(); };
  constexpr int NST = SK / 32;

  auto step = [&](const int st, auto slc, auto modec) {
    constexpr int SL = slc, MODE = modec;
    constexpr bool FIRST = MODE == 0, more_v = MODE <= 2, more_k = MODE <= 1;
    if constexpr (BIG) { if constexpr (FIRST) wait_barrier<4>(); else wait_barrier<0>(); }
    else { if constexpr (FIRST || MODE == 3) wait_barrier<8>(); else if constexpr (more_v) wait_barrier<12>(); else wait_barrier<0>(); }
    const char* ks = lds + SL * SLOT;
    const char* vs = lds + ((SL + 3) & 3) * SLOT;
    const char* vn = lds + SL * SLOT;
    auto ring = [&](auto dc) {
      constexpr int d = dc;
      if constexpr (!BIG && d < 4 && more_v) dma_v(st + 2, (SL + 2) & 3, d >> 1, d & 1);
      if constexpr (!BIG && d >= 4 && more_k) dma_k(st + 3, (SL + 3) & 3, (d - 4) >> 1, d & 1);
    };
    bf16x8 kf[8], vf[8];
    f32x4 sa[2][NQ];
    constexpr int KPD = BIG ? KPD_BIG : 8, VPD = BIG ? VPD_BIG : VPD_SMALL;
    static_for<0, KPD>([&](auto i) { kf[i] = lds_read128(ks, aK[i & 3] + (i >> 2) * 4096); });
    SB();
    static_for<0, 8>([&](auto d) { ring(d); });
    SB();
    if constexpr (!FIRST) {
#pragma unroll
      for (int t = 0; t < NQ; ++t) mfma_acc(oa[0][t], vp0, pf[t]);
#pragma unroll
      for (int t = 0; t < NQ; ++t) mfma_acc(la[t], ones, pf[t]);
#pragma unroll
      for (int t = 0; t < NQ; ++t) mfma_acc(oa[1][t], vp1, pf[t]);
    }
    SB();
    float pmt[NQ];
    static_for<0, 8>([&](auto gc) {
      constexpr int gi = gc, kt = gi >> 2, s = gi & 3;
      if constexpr (gi + KPD < 8) kf[gi + KPD] = lds_read128(ks, aK[(gi + KPD) & 3] + ((gi + KPD) >> 2) * 4096);
      if constexpr (!FIRST && gi >= 8 - VPD) vf[gi - (8 - VPD) + 2] = rdv(vs, gi - (8 - VPD) + 2);
#pragma unroll
      for (int t = 0; t < NQ; ++t) {
        if constexpr (s > 0) mfma_acc(sa[kt][t], kf[gi], qf[t][s]);
        else if constexpr (FIRST) mfma_zero(sa[kt][t], kf[gi], qf[t][s]);
        else mfma_new(sa[kt][t], kf[gi], qf[t][s], nm[t]);
      }
      if constexpr (gi == 7) {
#pragma unroll
        for (int t = 0; t < NQ; ++t) pmt[t] = max3_s(sa[0][t][0], sa[0][t][1], sa[0][t][2]);
      }
      SB();
    });
    if constexpr (!FIRST) {
      if constexpr (2 + VPD < 8) vf[2 + VPD] = rdv(vs, 2 + VPD);
#pragma unroll
      for (int t = 0; t < NQ; ++t) mfma_acc(oa[2][t], vf[2], pf[t]);
    }
    asm volatile("s_nop 4" ::: "memory");
    SB();
#pragma unroll
    for (int t = 0; t < NQ; ++t) pmt[t] = max3_s(pmt[t], sa[0][t][3], sa[1][t][0]);
#pragma unroll
    for (int t = 0; t < NQ; ++t) pmt[t] = max3_s(pmt[t], sa[1][t][1], sa[1][t][2]);
    float pm = max3_s(pmt[0], sa[1][0][3], sa[1][1][3]);
    if constexpr (NQ == 3) pm = max3_s(pm, pmt[2], sa[1][2][3]);
    pm = fmaxf(pm, pmt[1]);
    SB();
    bool pend = false;
    u32x4 pn[NQ];
    if (FIRST || __builtin_expect(__any(pm > RESCALE_THR), 0)) {
#pragma unroll
      for (int t = 0; t < NQ; ++t) {
        float rm = fmaxf(fmaxf(fmaxf(sa[0][t][0], sa[0][t][1]), fmaxf(sa[0][t][2], sa[0][t][3])),
                         fmaxf(fmaxf(sa[1][t][0], sa[1][t][1]), fmaxf(sa[1][t][2], sa[1][t][3])));
        rm = xmax16_32(rm);
        const float dl = FIRST ? rm : fmaxf(rm, 0.f);
        if constexpr (!FIRST) facs()[t * 64] = __builtin_amdgcn_exp2f(-dl);
        const f32x2 dd = splat2(dl);
        if constexpr (FIRST) {
          nm[t] = sub4(f32x4{0.f, 0.f, 0.f, 0.f}, dd);
          sa[0][t] = sub4(sa[0][t], dd);
          sa[1][t] = sub4(sa[1][t], dd);
        } else {
          const bf16x8 z = zero_frag();
          mfma_move(nm[t], sub4(nm[t], dd), z);
          mfma_move(sa[0][t], sub4(sa[0][t], dd), z);
          mfma_move(sa[1][t], sub4(sa[1][t], dd), z);
        }
      }
      if constexpr (!FIRST) mfma_settle();
      pend = !FIRST;
    }
    SB();
    static_for<3, 8>([&](auto dc) {
      constexpr int dt = dc;
      if constexpr (!FIRST && dt + VPD < 8) vf[dt + VPD] = rdv(vs, dt + VPD);
      if constexpr (dt == 6) vp0 = rdv(vn, 0);
      if constexpr (dt == 7) vp1 = rdv(vn, 1);
      if constexpr (!FIRST) {
#pragma unroll
        for (int t = 0; t < NQ; ++t) mfma_acc(oa[dt][t], vf[dt], pf[t]);
      }
      constexpr int e0 = (4 * NQ * (dt - 3)) / 5, e1 = (4 * NQ * (dt - 2)) / 5;
#pragma unroll
      for (int e = e0; e < e1; ++e) {
        const f32x4 sv = sa[(e >> 1) & 1][e >> 2];
        unsigned pk = cvtpk(__builtin_amdgcn_exp2f(sv[2 * (e & 1)]), __builtin_amdgcn_exp2f(sv[2 * (e & 1) + 1]));
        asm volatile("" : "+v"(pk));
        pn[e >> 2][e & 3] = pk;
      }
      SB();
    });
    if (__builtin_expect(pend, 0)) {
      mfma_settle();
#pragma unroll
      for (int t = 0; t < NQ; ++t) {
        const f32x2 ff = splat2(facs()[t * 64]);
        const bf16x8 z = zero_frag();
        mfma_move(la[t], mul4(la[t], ff), z);
#pragma unroll
        for (int dt = 0; dt < 8; ++dt) mfma_move(oa[dt][t], mul4(oa[dt][t], ff), z);
      }
    }
#pragma unroll
    for (int t = 0; t < NQ; ++t) pf[t] = __builtin_bit_cast(bf16x8, pn[t]);
    SB();
  };
  using I0 = std::integral_constant<int, 0>; using I1 = std::integral_constant<int, 1>;
  using I2 = std::integral_constant<int, 2>; using I3 = std::integral_constant<int, 3>; using I4 = std::integral_constant<int, 4>;
  step(0, I0{}, I0{});
  for (int st = 1; st < NST - 3; st += 4) {
    step(st, I1{}, I1{});
    step(st + 1, I2{}, I1{});
    step(st + 2, I3{}, I1{});
    step(st + 3, I0{}, I1{});
  }
  step(NST - 3, I1{}, I2{});
  step(NST - 2, I2{}, I3{});
  step(NST - 1, I3{}, I4{});
  {
    const char* vs = lds + ((NST - 1) & 3) * SLOT;
    bf16x8 vf[8];
#pragma unroll
    for (int dt = 2; dt < 8; ++dt) vf[dt] = rdv(vs, dt);
#pragma unroll
    for (int t = 0; t < NQ; ++t) {
      mfma_acc(la[t], ones, pf[t]);
      mfma_acc(oa[0][t], vp0, pf[t]);
      mfma_acc(oa[1][t], vp1, pf[t]);
    }
#pragma unroll
    for (int dt = 2; dt < 8; ++dt)
#pragma unroll
      for (int t = 0; t < NQ; ++t) mfma_acc(oa[dt][t], vf[dt], pf[t]);
    mfma_settle();
  }

  const unsigned lane2 = lane_id();
  const srd_t srdO = make_srd(ows + WS_O + ((size_t)(rowtile0 + qt0) * 128 + (head0 + hh) * 4) * 1024, NQ * 128 * 1024);
#pragma unroll
  for (int t = 0; t < NQ; ++t) {
    const f32x2 inv = splat2(1.0f / la[t][0]);
#pragma unroll
    for (int u = 0; u < 4; ++u) {
      const f32x4 o0 = mul4(oa[2 * u][t], inv), o1 = mul4(oa[2 * u + 1][t], inv);
      const u32x4 o = {cvtpk(o0[0], o0[1]), cvtpk(o0[2], o0[3]), cvtpk(o1[0], o1[1]), cvtpk(o1[2], o1[3])};
      __builtin_amdgcn_raw_buffer_store_b128(o, srdO, lane2 * 16 + (t * 128 + u) * 1024, 0, 16);
    }
  }
}

constexpr unsigned P2_STB = 40960, P2_O = 24576, P2_TOUCH_LDS = 3 * P2_STB;

DI bool flags_ready(const gu64* f, unsigned long long tag) { return __all(__hip_atomic_load(f, __ATOMIC_RELAXED, __HIP_MEMORY_SCOPE_AGENT) == tag); }
DI void wait_flags(const gu64* f, unsigned long long tag) {
  for (unsigned spins = 0; !flags_ready(f, tag) && spins < SPIN_CAP; ++spins) __builtin_amdgcn_s_sleep(1);
}
DI void dma16_agent(srd_t srd, unsigned voff, unsigned soff, unsigned ldsaddr) {
  asm volatile("s_mov_b32 m0, %0\n\ts_nop 0\n\tbuffer_load_dwordx4 %1, %2, %3 offen sc1 lds" ::"s"(ldsaddr), "v"(voff), "s"(srd), "s"(soff) : "memory");
}

DI void oproj(char* lds, const int w, const int lane, const char* wo, char* ows, char* out, const int branch, const int j, const int idx,
              gu64* flags, const unsigned long long tag) {
  const int rb = idx / 3, cb = idx % 3;
  const int g = lane >> 4, l15 = lane & 15;
  const unsigned ldsb = (unsigned)(size_t)lds, tsc = ldsb + P2_TOUCH_LDS;
  const srd_t srdW = make_srd(wo + (size_t)j * 1024 * (DM * 2) + cb * 384, 1023 * (DM * 2) + 384);
  const srd_t srdO = make_srd(ows + WS_O + ((size_t)(branch * 80 + rb * 8) * 128 + 32 * j) * 1024, (7 * 128 + 32) * 1024);
  const unsigned r8 = (lane >> 2) & 7;
  const unsigned vW = r8 * (DM * 2) + (lane >> 5) * 64 + 16 * ((lane & 3) ^ (2 * (r8 >> 2))), vO = lane * 16;
  auto dma_w = [&](int s, int i) { dma16<0>(srdW, vW, (s * 64 + 8 * w) * (DM * 2) + i * 128, ldsb + (s % 3) * P2_STB + (w * 3 + i) * 1024); };
  auto dma_o = [&](int s, int i) { dma16_agent(srdO, vO, (w * 128 + 2 * s + i) * 1024, ldsb + (s % 3) * P2_STB + P2_O + (w * 2 + i) * 1024); };
  static_for<0, 3>([&](auto i) { dma_w(0, i); });
  static_for<0, 3>([&](auto i) { dma_w(1, i); });

  if (w == 0) {
    const int blo = (rb * 128) / SQ, bhi = (rb * 128 + 127) / SQ;
    wait_flags(flags + (branch * NB + ((lane & 4) ? bhi : blo)) * 16 + j * 4 + (lane & 3), tag);
  }
  asm volatile("s_barrier" ::: "memory");
  if (P2_BURST) {
    const char* ob = ows + WS_O + ((size_t)(branch * 80 + rb * 8) * 128 + 32 * j) * 1024;
    const unsigned l0 = lane + 64 * cb;
    touch<2>(ob + (size_t)w * 131072 + l0 * 128, tsc);
    if (lane < 22) { const unsigned l1 = 192 + 22 * cb + lane; touch<2>(ob + (size_t)w * 131072 + (l1 < 256 ? l1 : 255) * 128, tsc); }
  }
  static_for<0, 2>([&](auto i) { dma_o(0, i); });
  static_for<0, 2>([&](auto i) { dma_o(1, i); });

  const int wm = w & 1, wn = w >> 1;
  const unsigned aWb = (g >> 1) * 3072 + (4 * (g & 1) + ((lane >> 2) & 3)) * 64 + 16 * (((lane & 3) >> 1) ^ (2 * (g & 1))) + 8 * (lane & 1);
  unsigned aW[3];
#pragma unroll
  for (int n = 0; n < 3; ++n) { const unsigned nt = 3 * wn + n; aW[n] = (aWb ^ ((nt & 1) * 32)) + (nt >> 1) * 512; }
  const unsigned aO = P2_O + wm * 8192 + lane * 16;
  f32x4 acc[4][3];
  constexpr int NS = 16;
  bf16x8 od[4], wd[3];
  if (w >= 4) __builtin_amdgcn_s_setprio(1);
  static_for<0, NS>([&](auto sc) {
    constexpr int s = sc;
    if constexpr (s == 0) wait_barrier<2>(); else if constexpr (s + 1 < NS) wait_barrier<5>(); else wait_barrier<0>();
    const char* st = lds + (s % 3) * P2_STB;
    bf16x8 of[4], wf[3];
    auto rdw = [&](int kk, int n) { const unsigned a = aW[n] + kk * 4 * 3072; return lds_read_tr2(st, a, a + 2 * 3072); };
    auto rdo = [&](int kk, int m) { return lds_read128(st, aO + (m * 2 + kk) * 1024); };
    auto loads = [&]() {
      if constexpr (s + 2 < NS) {
        static_for<0, 3>([&](auto i) { dma_w(s + 2, i); });
        static_for<0, 2>([&](auto i) { dma_o(s + 2, i); });
      }
    };
    static_for<0, 3>([&](auto n) { wf[n] = rdw(0, n); });
    static_for<0, 4>([&](auto m) { of[m] = rdo(0, m); });
    SB();
    if (w < 4) loads();
    SB();
    if constexpr (s > 0) {
      static_for<0, 4>([&](auto m) { static_for<0, 3>([&](auto n) { mfma_acc(acc[m][n], wd[n], od[m]); }); SB(); });
    }
    static_for<0, 4>([&](auto mc) {
      constexpr int m = mc;
      static_for<0, 3>([&](auto n) { mfma_az<s == 0>(acc[m][n], wf[n], of[m]); });
      if constexpr (m == 0) static_for<0, 3>([&](auto n) { wd[n] = rdw(1, n); });
      if constexpr (m == 1) static_for<0, 4>([&](auto mm) { od[mm] = rdo(1, mm); });
      SB();
    });
    if (w >= 4) loads();
    SB();
  });
  static_for<0, 4>([&](auto m) { static_for<0, 3>([&](auto n) { mfma_acc(acc[m][n], wd[n], od[m]); }); });
  mfma_settle();

  char* pbase = ows + WS_PART + (size_t)((branch * 10 + rb) * 3 + cb) * (16 * 12288);
  {
    const srd_t srdP = make_srd(pbase + (size_t)j * (4 * 12288), 4 * 12288);
#pragma unroll
    for (int h = 0; h < 2; ++h) {
      const int q = 2 * wm + h;
      if (q != j) {
#pragma unroll
        for (int n = 0; n < 3; ++n) {
          const f32x4 a0 = acc[2 * h][n], a1 = acc[2 * h + 1][n];
          const u32x4 v = {cvtpk(a0[0], a0[1]), cvtpk(a0[2], a0[3]), cvtpk(a1[0], a1[1]), cvtpk(a1[2], a1[3])};
          __builtin_amdgcn_raw_buffer_store_b128(v, srdP, lane * 16 + q * 12288 + (3 * wn + n) * 1024, 0, 16);
        }
      }
    }
  }
  gu64* flags2 = flags + 512 + ((branch * 10 + rb) * 3 + cb) * 4;
  asm volatile("s_waitcnt vmcnt(0)\n\ts_barrier" ::: "memory");
  if (w == 0 && lane == 0) __hip_atomic_store(flags2 + j, tag, __ATOMIC_RELAXED, __HIP_MEMORY_SCOPE_AGENT);
  if (wm == (j >> 1)) {
    wait_flags(flags2 + ((lane & 3) == j ? (j ^ 1) : (lane & 3)), tag);
    const srd_t srdP = make_srd(pbase, 16 * 12288);
    u32x4 pv[3][3];
#pragma unroll
    for (int q = 0; q < 3; ++q) {
      const int src = (j + 1 + q) & 3;
#pragma unroll
      for (int n = 0; n < 3; ++n) pv[q][n] = __builtin_amdgcn_raw_buffer_load_b128(srdP, lane * 16 + (3 * wn + n) * 1024, (src * 4 + j) * 12288, 16);
    }
#pragma unroll
    for (int i = 0; i < 6; ++i) {
      const int mm = i / 3, n = i % 3;
      f32x4 a = (j & 1) ? acc[2 + mm][n] : acc[mm][n];
#pragma unroll
      for (int q = 0; q < 3; ++q) {
        a[0] += __uint_as_float(pv[q][n][2 * mm] << 16);
        a[1] += __uint_as_float(pv[q][n][2 * mm] & 0xFFFF0000u);
        a[2] += __uint_as_float(pv[q][n][2 * mm + 1] << 16);
        a[3] += __uint_as_float(pv[q][n][2 * mm + 1] & 0xFFFF0000u);
      }
      const int row = rb * 128 + 32 * j + 16 * mm + l15;
      const u32x2 o = {cvtpk(a[0], a[1]), cvtpk(a[2], a[3])};
      *reinterpret_cast<u32x2*>(out + (size_t)row * (2 * DM * 2) + (branch * DM + cb * 192 + (3 * wn + n) * 16 + 4 * g) * 2) = o;
    }
  }
}

__global__ __launch_bounds__(512, 2) void fused_kernel(const char* __restrict__ xA, const char* __restrict__ kA, const char* __restrict__ vA,
                                                        const char* __restrict__ wqA, const char* __restrict__ woA,
                                                        const char* __restrict__ xB, const char* __restrict__ kB, const char* __restrict__ vB,
                                                        const char* __restrict__ wqB, const char* __restrict__ woB,
                                                        char* __restrict__ out, char* __restrict__ ows) {
  extern __shared__ __attribute__((aligned(16))) char lds[];
  const int w = (int)rfl(threadIdx.x >> 6);
  const int xcd = blockIdx.x & 7, idx = blockIdx.x >> 3;
  const int branch = xcd >> 2, j = xcd & 3;
  gu64* flags = (gu64*)(ows + WS_FLAGS);
  gu64* epoch = (gu64*)(ows + WS_EPOCH);
  unsigned long long tag;
  {
    const int b = idx >> 2, head0 = 8 * j + 2 * (idx & 3);
    const char* xb = (branch ? xB : xA) + (size_t)b * SQ * DM * 2;
    const char* wqb = (branch ? wqB : wqA) + head0 * DH * 2;
    const size_t kvo = (size_t)(b * NH + head0) * SK * DH * 2;
    const char* k = (branch ? kB : kA) + kvo;
    const char* v = (branch ? vB : vA) + kvo;
    const int rowtile0 = (branch * NB + b) * 10;
    const char* wqt = (branch ? wqB : wqA) + (size_t)idx * 18 * HD * 2 + j * 2048;
    const char* xt = (branch ? xB : xA) + (size_t)idx * 46080;
    if (w < 4) attn_wave<3>(lds, w, (int)lane_id(), xb, wqb, k, v, ows, rowtile0, head0, wqt, xt);
    else attn_wave<2>(lds, w, (int)lane_id(), xb, wqb, k, v, ows, rowtile0, head0, wqt, xt);
    {
      const unsigned long long e = __hip_atomic_load(epoch, __ATOMIC_RELAXED, __HIP_MEMORY_SCOPE_AGENT) + TAG_STEP;
      tag = ((unsigned long long)rfl((unsigned)(e >> 32)) << 32) | rfl((unsigned)e);
    }
    asm volatile("s_waitcnt vmcnt(0) lgkmcnt(0)\n\ts_barrier" ::: "memory");
    if (w == 0 && lane_id() == 0) __hip_atomic_store(flags + (branch * NB + b) * 16 + j * 4 + (idx & 3), tag, __ATOMIC_RELAXED, __HIP_MEMORY_SCOPE_AGENT);
  }
  const int lane = (int)lane_id();
  const char* wo = branch ? woB : woA;
  if (P2_BURST) {
    const char* p = wo + (size_t)j * 1024 * (DM * 2) + (size_t)(idx * 288 + w * 36) * 128;
    if (lane < 36) touch<0>(p + lane * 128, (unsigned)(size_t)lds + P2_TOUCH_LDS);
  }
  if (idx < 30) {
    oproj(lds, w, lane, wo, ows, out, branch, j, idx, flags, tag);
  } else if (blockIdx.x == 255 && w == 0) {
    bool ok = false;
    for (unsigned spins = 0; !ok && spins < SPIN_CAP; ++spins) {
      ok = flags_ready(flags + lane, tag) && flags_ready(flags + 64 + lane, tag) && flags_ready(flags + 128 + lane, tag) && flags_ready(flags + 192 + lane, tag);
      if (!ok) __builtin_amdgcn_s_sleep(8);
    }
    if (lane == 0) __hip_atomic_store(epoch, tag, __ATOMIC_RELAXED, __HIP_MEMORY_SCOPE_AGENT);
  }
}

extern "C" void kernel_launch(void* const* d_in, const int* in_sizes, int n_in, void* d_out, int out_size, void* d_ws, size_t ws_size,
                              hipStream_t stream) {
  (void)in_sizes; (void)n_in; (void)out_size; (void)ws_size;
  (void)hipFuncSetAttribute((const void*)fused_kernel, hipFuncAttributeMaxDynamicSharedMemorySize, LDS_BYTES);
  fused_kernel<<<256, 512, LDS_BYTES, stream>>>((const char*)d_in[0], (const char*)d_in[1], (const char*)d_in[2], (const char*)d_in[3],
                                                (const char*)d_in[4], (const char*)d_in[5], (const char*)d_in[6], (const char*)d_in[7],
                                                (const char*)d_in[8], (const char*)d_in[9], (char*)d_out, (char*)d_ws);
}
```

```cpp
#include <hip/hip_runtime.h>
#include <hip/hip_bf16.h>

typedef short s16x4 __attribute__((ext_vector_type(4)));
typedef short s16x8 __attribute__((ext_vector_type(8)));
typedef __bf16 bf16x2 __attribute__((ext_vector_type(2)));
typedef __bf16 bf16x8 __attribute__((ext_vector_type(8)));
typedef float f32x2 __attribute__((ext_vector_type(2)));
typedef float f32x4 __attribute__((ext_vector_type(4)));
typedef unsigned u32x2 __attribute__((ext_vector_type(2)));
typedef unsigned u32x4 __attribute__((ext_vector_type(4)));
typedef __attribute__((address_space(3))) s16x4* lds_s16x4_ptr;
typedef __attribute__((address_space(1))) unsigned long long gu64;
typedef __amdgpu_buffer_rsrc_t srd_t;

#define DI __device__ __forceinline__
#define SB() __builtin_amdgcn_sched_barrier(0)

constexpr int NB = 8, SQ = 160, DM = 576, NH = 32, SK = 512, DH = 128;
constexpr int HD = NH * DH;
constexpr float SCALE_LOG2E = 0.08838834764831845f * 1.4426950408889634f;
constexpr float RESCALE_THR = 8.0f;
constexpr unsigned long long TAG_STEP = 0x9E3779B97F4A7C15ull;
constexpr unsigned SPIN_CAP = 1u << 20;

constexpr size_t WS_O = 0;
constexpr size_t WS_PART = (size_t)160 * 128 * 1024;
constexpr size_t WS_FLAGS = WS_PART + (size_t)60 * 16 * 12288;
constexpr size_t WS_EPOCH = WS_FLAGS + 8192;

#define KV_NT 1
#define KPD_BIG 2
#define VPD_BIG 2
#define VPD_SMALL 4
#define P2_BURST 1
#define KVT_0 2
#define KVT_N 0
#define TOUCH_Q 0
#define TOUCH_NT 0
#define Q_NT 0

DI unsigned rfl(unsigned x) { return __builtin_amdgcn_readfirstlane(x); }
DI srd_t make_srd(const void* p, unsigned bytes) { return __builtin_amdgcn_make_buffer_rsrc((void*)p, (short)0, (int)bytes, 0x00020000); }

template <int NT>
DI void dma16(srd_t srd, unsigned voff, unsigned soff, unsigned ldsaddr) {
  if constexpr (NT)
    asm volatile("s_mov_b32 m0, %0\n\ts_nop 0\n\tbuffer_load_dwordx4 %1, %2, %3 offen nt lds" ::"s"(ldsaddr), "v"(voff), "s"(srd), "s"(soff) : "memory");
  else
    asm volatile("s_mov_b32 m0, %0\n\ts_nop 0\n\tbuffer_load_dwordx4 %1, %2, %3 offen lds" ::"s"(ldsaddr), "v"(voff), "s"(srd), "s"(soff) : "memory");
}
template <int POL>
DI void touch(const char* p, unsigned ldsaddr) {
  if constexpr (POL == 1) asm volatile("s_mov_b32 m0, %0\n\ts_nop 0\n\tglobal_load_lds_dword %1, off nt" ::"s"(ldsaddr), "v"(p) : "memory");
  else if constexpr (POL == 2) asm volatile("s_mov_b32 m0, %0\n\ts_nop 0\n\tglobal_load_lds_dword %1, off sc1" ::"s"(ldsaddr), "v"(p) : "memory");
  else asm volatile("s_mov_b32 m0, %0\n\ts_nop 0\n\tglobal_load_lds_dword %1, off" ::"s"(ldsaddr), "v"(p) : "memory");
}
template <int N>
DI void wait_barrier() { asm volatile("s_waitcnt vmcnt(%0) lgkmcnt(0)\n\ts_barrier" ::"i"(N) : "memory"); }

DI unsigned cvtpk(float lo, float hi) {
  f32x2 v = {lo, hi};
  bf16x2 b = __builtin_convertvector(v, bf16x2);
  return __builtin_bit_cast(unsigned, b);
}
DI void mfma_acc(f32x4& c, bf16x8 a, bf16x8 b) { asm volatile("v_mfma_f32_16x16x32_bf16 %0, %1, %2, %0" : "+v"(c) : "v"(a), "v"(b)); }
DI void mfma_new(f32x4& d, bf16x8 a, bf16x8 b, f32x4 c) { asm volatile("v_mfma_f32_16x16x32_bf16 %0, %1, %2, %3" : "=&v"(d) : "v"(a), "v"(b), "v"(c)); }
DI void mfma_zero(f32x4& d, bf16x8 a, bf16x8 b) { asm volatile("v_mfma_f32_16x16x32_bf16 %0, %1, %2, 0" : "=&v"(d) : "v"(a), "v"(b)); }
template <bool ZERO>
DI void mfma_az(f32x4& c, bf16x8 a, bf16x8 b) { if constexpr (ZERO) mfma_zero(c, a, b); else mfma_acc(c, a, b); }
DI void mfma_settle() { asm volatile("s_nop 7\n\ts_nop 1" ::: "memory"); __builtin_amdgcn_sched_barrier(0); }
DI bf16x8 lds_read128(const char* lds, unsigned off) { return *reinterpret_cast<const bf16x8*>(lds + off); }
DI bf16x8 lds_read_tr2(const char* lds, unsigned off0, unsigned off1) {
  s16x4 a = __builtin_amdgcn_ds_read_tr16_b64_v4i16((lds_s16x4_ptr)(lds + off0));
  s16x4 b = __builtin_amdgcn_ds_read_tr16_b64_v4i16((lds_s16x4_ptr)(lds + off1));
  s16x8 r = __builtin_shufflevector(a, b, 0, 1, 2, 3, 4, 5, 6, 7);
  return __builtin_bit_cast(bf16x8, r);
}
DI f32x2 splat2(float v) { f32x2 p = {v, v}; asm volatile("" : "+v"(p)); return p; }
DI f32x4 mul4(f32x4 a, f32x2 p) {
  f32x2 lo = f32x2{a[0], a[1]} * p, hi = f32x2{a[2], a[3]} * p;
  return f32x4{lo[0], lo[1], hi[0], hi[1]};
}
DI f32x4 sub4(f32x4 a, f32x2 p) {
  f32x2 lo = f32x2{a[0], a[1]} - p, hi = f32x2{a[2], a[3]} - p;
  return f32x4{lo[0], lo[1], hi[0], hi[1]};
}
DI void mfma_move(f32x4& dst, f32x4 src, bf16x8 zero) { asm volatile("s_nop 1\n\tv_mfma_f32_16x16x32_bf16 %0, %1, %1, %2" : "+v"(dst) : "v"(zero), "v"(src)); }
DI bf16x8 zero_frag() { u32x4 z = {0u, 0u, 0u, 0u}; asm volatile("" : "+v"(z)); return __builtin_bit_cast(bf16x8, z); }
DI float max3_s(float a, float b, float c) { float r; asm volatile("v_max3_f32 %0, %1, %2, %3" : "=v"(r) : "v"(a), "v"(b), "v"(c)); return r; }
DI float xmax16_32(float v) {
  unsigned u = __float_as_uint(v);
  auto a = __builtin_amdgcn_permlane16_swap(u, u, false, false);
  v = fmaxf(__uint_as_float(a[0]), __uint_as_float(a[1]));
  u = __float_as_uint(v);
  auto b = __builtin_amdgcn_permlane32_swap(u, u, false, false);
  return fmaxf(__uint_as_float(b[0]), __uint_as_float(b[1]));
}
DI unsigned lane_id() { unsigned l; asm volatile("v_mbcnt_lo_u32_b32 %0, -1, 0\n\tv_mbcnt_hi_u32_b32 %0, -1, %0" : "=v"(l)); return l; }
template <int I, int N, class F>
DI void static_for(F&& f) {
  if constexpr (I < N) { f(std::integral_constant<int, I>{}); static_for<I + 1, N>(f); }
}

constexpr unsigned KRING = 0, VRING = 65536, SLOT = 16384, QST_X = 16384;
constexpr unsigned qstage(int i) { return i == 0 ? 32768u : (i == 1 ? 98304u : 124928u); }
constexpr unsigned FAC_LDS = 151552, TOUCH_LDS = 157696;
constexpr int LDS_BYTES = 163840;

template <int NQ>
DI void attn_wave(char* lds, const int w, const int lane, const char* xb, const char* wqb, const char* kb0, const char* vb0,
                  char* ows, const int rowtile0, const int head0, const char* wq_touch, const char* x_touch) {
  constexpr bool BIG = (NQ == 3);
  const int hh = (w >> 1) & 1, wi = w & 1, j4 = w & 3;
  const int g = lane >> 4, l15 = lane & 15;
  const int qt0 = BIG ? 3 * wi : 6 + 2 * wi;
  const unsigned ldsb = (unsigned)(size_t)lds;

  const srd_t srdK0 = make_srd(kb0, SK * DH * 2), srdK1 = make_srd(kb0 + SK * DH * 2, SK * DH * 2);
  const srd_t srdV0 = make_srd(vb0, SK * DH * 2), srdV1 = make_srd(vb0 + SK * DH * 2, SK * DH * 2);
  const unsigned krow = 4 * j4 + g;
  const unsigned vK = krow * 256 + 16 * (l15 ^ krow);
  const unsigned tr_row = (lane >> 2) & 7;
  const unsigned vV = tr_row * 256 + (lane >> 5) * 64 + 16 * ((lane & 3) ^ (2 * (tr_row >> 2)));
  auto dma_k = [&](int step, int slot, int i, int head) {
    dma16<KV_NT>(head ? srdK1 : srdK0, vK, step * 8192 + i * 4096, ldsb + KRING + slot * SLOT + head * 8192 + (j4 + 4 * i) * 1024);
  };
  auto dma_v = [&](int step, int slot, int i, int head) {
    dma16<KV_NT>(head ? srdV1 : srdV0, vV, step * 8192 + j4 * 2048 + i * 128, ldsb + VRING + slot * SLOT + head * 8192 + (2 * j4 + i) * 1024);
  };
  auto issue_k = [&](int step) { dma_k(step, step & 3, 0, 0); dma_k(step, step & 3, 0, 1); dma_k(step, step & 3, 1, 0); dma_k(step, step & 3, 1, 1); };
  auto issue_v = [&](int step) { dma_v(step, step & 3, 0, 0); dma_v(step, step & 3, 0, 1); dma_v(step, step & 3, 1, 0); dma_v(step, step & 3, 1, 1); };

  const srd_t srdX = make_srd(xb, SQ * DM * 2), srdW = make_srd(wqb, (DM - 1) * HD * 2 + 512);
  const unsigned vX = (lane >> 2) * (DM * 2) + 16 * ((lane & 3) ^ ((0u - (lane >> 4)) & 3));
  const unsigned vWp = (tr_row + 8 * j4) * (HD * 2) + (lane >> 5) * 64 + 16 * ((lane & 3) ^ (2 * (j4 & 1)));
  const unsigned kbw = w >> 1;
  const unsigned vW = (tr_row + 8 * kbw) * (HD * 2) + (lane >> 5) * 64 + 16 * ((lane & 3) ^ (2 * (kbw & 1))) + (w & 1) * 256;
  const unsigned xt2 = rfl(j4 < 2 ? j4 + 8 : 9);
  const unsigned xso2 = rfl(xt2 * 16 * (DM * 2));
  auto dma_q0 = [&](int ks, int i) {
    const unsigned lb = ldsb + qstage(ks % 3);
    if (i < 4) dma16<Q_NT>(srdW, vWp, ks * 32 * (HD * 2) + i * 128, lb + (j4 * 4 + i) * 1024);
    else if (i < 6) dma16<Q_NT>(srdX, vX, (j4 + 4 * (i - 4)) * 16 * (DM * 2) + ks * 64, lb + QST_X + (j4 + 4 * (i - 4)) * 1024);
    else dma16<Q_NT>(srdX, vX, xso2 + ks * 64, lb + QST_X + xt2 * 1024);
  };
  auto dma_q = [&](int ks, int buf, int i) {
    const unsigned lb = ldsb + qstage(buf);
    if (i < 2) dma16<Q_NT>(srdW, vW, ks * 32 * (HD * 2) + i * 128, lb + (kbw * 4 + 2 * (w & 1) + i) * 1024);
    else if (i == 2) dma16<Q_NT>(srdX, vX, w * 16 * (DM * 2) + ks * 64, lb + QST_X + w * 1024);
    else if (!BIG) dma16<Q_NT>(srdX, vX, xso2 + ks * 64, lb + QST_X + xt2 * 1024);
  };
  constexpr int NKS = DM / 32;
  if constexpr (!BIG) {
    static_for<0, 7>([&](auto i) { dma_q0(0, i); });
    static_for<0, 7>([&](auto i) { dma_q0(1, i); });
  } else if constexpr (TOUCH_Q) {
    const unsigned l0 = j4 * 72 + lane, l1 = j4 * 72 + 64 + (lane & 7), sc = ldsb + TOUCH_LDS;
    touch<TOUCH_NT>(wq_touch + (l0 >> 4) * (HD * 2) + (l0 & 15) * 128, sc);
    touch<TOUCH_NT>(x_touch + (j4 * 90 + lane) * 128, sc);
    touch<TOUCH_NT>(lane < 8 ? wq_touch + (l1 >> 4) * (HD * 2) + (l1 & 15) * 128 : x_touch + (j4 * 90 + 64 + (lane < 34 ? lane - 8 : 0)) * 128, sc);
  }

  auto prefetch_kv = [&](int tile) {
    if constexpr (BIG) { dma_k(tile, tile, 0, 0); dma_k(tile, tile, 0, 1); dma_k(tile, tile, 1, 0); dma_k(tile, tile, 1, 1); }
    else { dma_v(tile, tile, 0, 0); dma_v(tile, tile, 0, 1); dma_v(tile, tile, 1, 0); dma_v(tile, tile, 1, 1); }
  };
  auto touch_kv = [&](int n) {
    const int u = n >> 1, hd = n & 1;
    const char* base = ((u & 1) ? kb0 : vb0) + hd * (SK * DH * 2) + (2 + ((u + 1) >> 1)) * 8192;
    if (lane < 16) touch<1>(base + (j4 * 16 + lane) * 128, ldsb + TOUCH_LDS);
  };
  f32x4 qa[8][NQ];
  {
    const unsigned aW0 = g * 4096 + hh * 2048 + ((lane >> 2) & 3) * 64 + 16 * (((lane & 3) >> 1) ^ (2 * (g & 1))) + 8 * (lane & 1);
    const unsigned aW1 = aW0 ^ 32;
    const unsigned aX = QST_X + qt0 * 1024 + l15 * 64 + 16 * (g ^ ((0u - (l15 >> 2)) & 3));
    bf16x8 xo[NQ], wd[3];
#pragma unroll
    for (int i = 0; i < 3; ++i) wd[i] = zero_frag();
#pragma unroll
    for (int t = 0; t < NQ; ++t) xo[t] = zero_frag();
#pragma unroll
    for (int dt = 0; dt < 8; ++dt)
#pragma unroll
      for (int t = 0; t < NQ; ++t) { qa[dt][t] = f32x4{0.f, 0.f, 0.f, 0.f}; asm volatile("" : "+v"(qa[dt][t])); }
    auto qstep = [&](const int ks, auto bufc) {
      constexpr int BUF = bufc;
      if constexpr (BIG) {
        if (ks < 2) wait_barrier<63>();
        else if (ks + 1 == NKS) wait_barrier<4 + (KVT_0 + KVT_N >= NKS - 2 ? 1 : 0)>();
        else if (ks - 2 >= KVT_0 && ks - 1 < KVT_0 + KVT_N) wait_barrier<5>();
        else if (ks - 1 >= KVT_0 && ks - 2 < KVT_0 + KVT_N) wait_barrier<4>();
        else wait_barrier<3>();
      }
      else { if (ks == 0) wait_barrier<7>(); else wait_barrier<4>(); }
      const char* st = lds + qstage(BUF);
      auto rdw = [&](int dt) { const unsigned a = ((dt & 1) ? aW1 : aW0) + (dt >> 1) * 512; return lds_read_tr2(st, a, a + 256); };
      bf16x8 xn[NQ], wf[8];
      const bool more = ks + 2 < NKS;
      if constexpr (!BIG) {
        static_for<0, NQ>([&](auto i) { xn[i] = lds_read128(st, aX + i * 1024); });
        static_for<0, 4>([&](auto i) { wf[i] = rdw(i); });
        SB();
        if (more) { static_for<0, 4>([&](auto i) { dma_q(ks + 2, (BUF + 2) % 3, i); }); }
        else prefetch_kv(ks - (NKS - 2));
        SB();
      }
      static_for<0, 3>([&](auto ic) {
        constexpr int i = ic;
        if constexpr (BIG) { xn[i] = lds_read128(st, aX + i * 1024); wf[i] = rdw(i); }
#pragma unroll
        for (int t = 0; t < NQ; ++t) mfma_acc(qa[5 + i][t], wd[i], xo[t]);
        SB();
      });
      constexpr int QPD = BIG ? 3 : 4;
      static_for<0, 5>([&](auto dtc) {
        constexpr int dt = dtc;
        if constexpr (dt + QPD < 8) wf[dt + QPD] = rdw(dt + QPD);
#pragma unroll
        for (int t = 0; t < NQ; ++t) mfma_acc(qa[dt][t], wf[dt], xn[t]);
        SB();
      });
      if constexpr (BIG) {
        if (more) { static_for<0, 3>([&](auto i) { dma_q(ks + 2, (BUF + 2) % 3, i); }); }
        else prefetch_kv(ks - (NKS - 2));
        if (ks >= KVT_0 && ks < KVT_0 + KVT_N) touch_kv(ks - KVT_0);
        SB();
      }
#pragma unroll
      for (int i = 0; i < 3; ++i) wd[i] = wf[5 + i];
#pragma unroll
      for (int t = 0; t < NQ; ++t) xo[t] = xn[t];
    };
    for (int ks = 0; ks < NKS; ks += 3) {
      qstep(ks, std::integral_constant<int, 0>{});
      qstep(ks + 1, std::integral_constant<int, 1>{});
      qstep(ks + 2, std::integral_constant<int, 2>{});
    }
#pragma unroll
    for (int i = 0; i < 3; ++i)
#pragma unroll
      for (int t = 0; t < NQ; ++t) mfma_acc(qa[5 + i][t], wd[i], xo[t]);
  }
  mfma_settle();
  wait_barrier<63>();
  bf16x8 qf[NQ][4];
  const f32x2 cc = splat2(SCALE_LOG2E);
#pragma unroll
  for (int t = 0; t < NQ; ++t)
#pragma unroll
    for (int s = 0; s < 4; ++s) {
      const f32x4 q0 = mul4(qa[2 * s][t], cc), q1 = mul4(qa[2 * s + 1][t], cc);
      unsigned r0a = cvtpk(q0[0], q0[1]), r0b = cvtpk(q0[2], q0[3]), r1a = cvtpk(q1[0], q1[1]), r1b = cvtpk(q1[2], q1[3]);
      auto a32 = __builtin_amdgcn_permlane32_swap(r0a, r1a, false, false);
      auto b32 = __builtin_amdgcn_permlane32_swap(r0b, r1b, false, false);
      auto a16 = __builtin_amdgcn_permlane16_swap(a32[0], a32[1], false, false);
      auto b16 = __builtin_amdgcn_permlane16_swap(b32[0], b32[1], false, false);
      u32x4 f = {a16[0], b16[0], a16[1], b16[1]};
      qf[t][s] = __builtin_bit_cast(bf16x8, f);
    }
  if constexpr (!BIG) issue_k(2);

  f32x4 oa[8][NQ], la[NQ], nm[NQ];
#pragma unroll
  for (int t = 0; t < NQ; ++t) {
    la[t] = f32x4{0.f, 0.f, 0.f, 0.f};
    asm volatile("" : "+v"(la[t]));
#pragma unroll
    for (int dt = 0; dt < 8; ++dt) { oa[dt][t] = f32x4{0.f, 0.f, 0.f, 0.f}; asm volatile("" : "+v"(oa[dt][t])); }
  }
  const u32x4 onesu = {0x3F803F80u, 0x3F803F80u, 0x3F803F80u, 0x3F803F80u};
  bf16x8 ones = __builtin_bit_cast(bf16x8, onesu);
  asm volatile("" : "+v"(ones));
  unsigned aK[4];
#pragma unroll
  for (int s = 0; s < 4; ++s) aK[s] = KRING + hh * 8192 + l15 * 256 + 16 * ((4 * s + g) ^ l15);
  const unsigned aV0 = VRING + hh * 8192 + (g >> 1) * 2048 + (4 * (g & 1) + ((lane >> 2) & 3)) * 64 + 16 * (((lane & 3) >> 1) ^ (2 * (g & 1))) + 8 * (lane & 1);
  const unsigned aV1 = aV0 ^ 32;
  auto rdv = [&](const char* vs, int dt) { const unsigned a = ((dt & 1) ? aV1 : aV0) + (dt >> 1) * 512; return lds_read_tr2(vs, a, a + 4096); };

  bf16x8 pf[NQ], vp0, vp1;
  auto facs = [&]() { return reinterpret_cast<float*>(lds + FAC_LDS) + w * 192 + lane_id(); };
  constexpr int NST = SK / 32;

  auto step = [&](const int st, auto slc, auto modec) {
    constexpr int SL = slc, MODE = modec;
    constexpr bool FIRST = MODE == 0, more_v = MODE <= 2, more_k = MODE <= 1;
    if constexpr (BIG) { if constexpr (FIRST) wait_barrier<4>(); else wait_barrier<0>(); }
    else { if constexpr (FIRST || MODE == 3) wait_barrier<8>(); else if constexpr (more_v) wait_barrier<12>(); else wait_barrier<0>(); }
    const char* ks = lds + SL * SLOT;
    const char* vs = lds + ((SL + 3) & 3) * SLOT;
    const char* vn = lds + SL * SLOT;
    auto ring = [&](auto dc) {
      constexpr int d = dc;
      if constexpr (!BIG && d < 4 && more_v) dma_v(st + 2, (SL + 2) & 3, d >> 1, d & 1);
      if constexpr (!BIG && d >= 4 && more_k) dma_k(st + 3, (SL + 3) & 3, (d - 4) >> 1, d & 1);
    };
    bf16x8 kf[8], vf[8];
    f32x4 sa[2][NQ];
    constexpr int KPD = BIG ? KPD_BIG : 8, VPD = BIG ? VPD_BIG : VPD_SMALL;
    static_for<0, KPD>([&](auto i) { kf[i] = lds_read128(ks, aK[i & 3] + (i >> 2) * 4096); });
    SB();
    static_for<0, 8>([&](auto d) { ring(d); });
    SB();
    if constexpr (!FIRST) {
#pragma unroll
      for (int t = 0; t < NQ; ++t) mfma_acc(oa[0][t], vp0, pf[t]);
#pragma unroll
      for (int t = 0; t < NQ; ++t) mfma_acc(la[t], ones, pf[t]);
#pragma unroll
      for (int t = 0; t < NQ; ++t) mfma_acc(oa[1][t], vp1, pf[t]);
    }
    SB();
    float pmt[NQ];
    static_for<0, 8>([&](auto gc) {
      constexpr int gi = gc, kt = gi >> 2, s = gi & 3;
      if constexpr (gi + KPD < 8) kf[gi + KPD] = lds_read128(ks, aK[(gi + KPD) & 3] + ((gi + KPD) >> 2) * 4096);
      if constexpr (!FIRST && gi >= 8 - VPD) vf[gi - (8 - VPD) + 2] = rdv(vs, gi - (8 - VPD) + 2);
#pragma unroll
      for (int t = 0; t < NQ; ++t) {
        if constexpr (s > 0) mfma_acc(sa[kt][t], kf[gi], qf[t][s]);
        else if constexpr (FIRST) mfma_zero(sa[kt][t], kf[gi], qf[t][s]);
        else mfma_new(sa[kt][t], kf[gi], qf[t][s], nm[t]);
      }
      if constexpr (gi == 7) {
#pragma unroll
        for (int t = 0; t < NQ; ++t) pmt[t] = max3_s(sa[0][t][0], sa[0][t][1], sa[0][t][2]);
      }
      SB();
    });
    if constexpr (!FIRST) {
      if constexpr (2 + VPD < 8) vf[2 + VPD] = rdv(vs, 2 + VPD);
#pragma unroll
      for (int t = 0; t < NQ; ++t) mfma_acc(oa[2][t], vf[2], pf[t]);
    }
    asm volatile("s_nop 4" ::: "memory");
    SB();
#pragma unroll
    for (int t = 0; t < NQ; ++t) pmt[t] = max3_s(pmt[t], sa[0][t][3], sa[1][t][0]);
#pragma unroll
    for (int t = 0; t < NQ; ++t) pmt[t] = max3_s(pmt[t], sa[1][t][1], sa[1][t][2]);
    float pm = max3_s(pmt[0], sa[1][0][3], sa[1][1][3]);
    if constexpr (NQ == 3) pm = max3_s(pm, pmt[2], sa[1][2][3]);
    pm = fmaxf(pm, pmt[1]);
    SB();
    bool pend = false;
    u32x4 pn[NQ];
    float ex[8 * NQ];
    if (FIRST || __builtin_expect(__any(pm > RESCALE_THR), 0)) {
#pragma unroll
      for (int t = 0; t < NQ; ++t) {
        float rm = fmaxf(fmaxf(fmaxf(sa[0][t][0], sa[0][t][1]), fmaxf(sa[0][t][2], sa[0][t][3])),
                         fmaxf(fmaxf(sa[1][t][0], sa[1][t][1]), fmaxf(sa[1][t][2], sa[1][t][3])));
        rm = xmax16_32(rm);
        const float dl = FIRST ? rm : fmaxf(rm, 0.f);
        if constexpr (!FIRST) facs()[t * 64] = __builtin_amdgcn_exp2f(-dl);
        const f32x2 dd = splat2(dl);
        if constexpr (FIRST) {
          nm[t] = sub4(f32x4{0.f, 0.f, 0.f, 0.f}, dd);
          sa[0][t] = sub4(sa[0][t], dd);
          sa[1][t] = sub4(sa[1][t], dd);
        } else {
          const bf16x8 z = zero_frag();
          mfma_move(nm[t], sub4(nm[t], dd), z);
          mfma_move(sa[0][t], sub4(sa[0][t], dd), z);
          mfma_move(sa[1][t], sub4(sa[1][t], dd), z);
        }
      }
      if constexpr (!FIRST) mfma_settle();
      pend = !FIRST;
    }
    SB();
    static_for<3, 8>([&](auto dc) {
      constexpr int dt = dc;
      if constexpr (!FIRST && dt + VPD < 8) vf[dt + VPD] = rdv(vs, dt + VPD);
      if constexpr (dt == 6) vp0 = rdv(vn, 0);
      if constexpr (dt == 7) vp1 = rdv(vn, 1);
      if constexpr (!FIRST) {
#pragma unroll
        for (int t = 0; t < NQ; ++t) mfma_acc(oa[dt][t], vf[dt], pf[t]);
      }
      constexpr int e0 = (4 * NQ * (dt - 3) + 3) / 4 > 4 * NQ ? 4 * NQ : (4 * NQ * (dt - 3) + 3) / 4;
      constexpr int e1 = (4 * NQ * (dt - 2) + 3) / 4 > 4 * NQ ? 4 * NQ : (4 * NQ * (dt - 2) + 3) / 4;
      constexpr int c0 = dt == 3 ? 0 : ((4 * NQ * (dt - 4) + 3) / 4 > 4 * NQ ? 4 * NQ : (4 * NQ * (dt - 4) + 3) / 4);
      static_for<c0, (dt == 3 ? 0 : e0)>([&](auto ec) {
        constexpr int e = ec;
        unsigned pk = cvtpk(ex[2 * e], ex[2 * e + 1]);
        asm volatile("" : "+v"(pk));
        pn[e >> 2][e & 3] = pk;
      });
      static_for<e0, e1>([&](auto ec) {
        constexpr int e = ec;
        const f32x4 sv = sa[(e >> 1) & 1][e >> 2];
        ex[2 * e] = __builtin_amdgcn_exp2f(sv[2 * (e & 1)]);
        ex[2 * e + 1] = __builtin_amdgcn_exp2f(sv[2 * (e & 1) + 1]);
        asm volatile("" : "+v"(ex[2 * e]), "+v"(ex[2 * e + 1]));
      });
      SB();
    });
    if (__builtin_expect(pend, 0)) {
      mfma_settle();
#pragma unroll
      for (int t = 0; t < NQ; ++t) {
        const f32x2 ff = splat2(facs()[t * 64]);
        const bf16x8 z = zero_frag();
        mfma_move(la[t], mul4(la[t], ff), z);
#pragma unroll
        for (int dt = 0; dt < 8; ++dt) mfma_move(oa[dt][t], mul4(oa[dt][t], ff), z);
      }
    }
#pragma unroll
    for (int t = 0; t < NQ; ++t) pf[t] = __builtin_bit_cast(bf16x8, pn[t]);
    SB();
  };
  using I0 = std::integral_constant<int, 0>; using I1 = std::integral_constant<int, 1>;
  using I2 = std::integral_constant<int, 2>; using I3 = std::integral_constant<int, 3>; using I4 = std::integral_constant<int, 4>;
  step(0, I0{}, I0{});
  for (int st = 1; st < NST - 3; st += 4) {
    step(st, I1{}, I1{});
    step(st + 1, I2{}, I1{});
    step(st + 2, I3{}, I1{});
    step(st + 3, I0{}, I1{});
  }
  step(NST - 3, I1{}, I2{});
  step(NST - 2, I2{}, I3{});
  step(NST - 1, I3{}, I4{});
  {
    const char* vs = lds + ((NST - 1) & 3) * SLOT;
    bf16x8 vf[8];
#pragma unroll
    for (int dt = 2; dt < 8; ++dt) vf[dt] = rdv(vs, dt);
#pragma unroll
    for (int t = 0; t < NQ; ++t) {
      mfma_acc(la[t], ones, pf[t]);
      mfma_acc(oa[0][t], vp0, pf[t]);
      mfma_acc(oa[1][t], vp1, pf[t]);
    }
#pragma unroll
    for (int dt = 2; dt < 8; ++dt)
#pragma unroll
      for (int t = 0; t < NQ; ++t) mfma_acc(oa[dt][t], vf[dt], pf[t]);
    mfma_settle();
  }

  const unsigned lane2 = lane_id();
  const srd_t srdO = make_srd(ows + WS_O + ((size_t)(rowtile0 + qt0) * 128 + (head0 + hh) * 4) * 1024, NQ * 128 * 1024);
#pragma unroll
  for (int t = 0; t < NQ; ++t) {
    const f32x2 inv = splat2(1.0f / la[t][0]);
#pragma unroll
    for (int u = 0; u < 4; ++u) {
      const f32x4 o0 = mul4(oa[2 * u][t], inv), o1 = mul4(oa[2 * u + 1][t], inv);
      const u32x4 o = {cvtpk(o0[0], o0[1]), cvtpk(o0[2], o0[3]), cvtpk(o1[0], o1[1]), cvtpk(o1[2], o1[3])};
      __builtin_amdgcn_raw_buffer_store_b128(o, srdO, lane2 * 16 + (t * 128 + u) * 1024, 0, 16);
    }
  }
}

constexpr unsigned P2_STB = 40960, P2_O = 24576, P2_TOUCH_LDS = 3 * P2_STB;

DI bool flags_ready(const gu64* f, unsigned long long tag) { return __all(__hip_atomic_load(f, __ATOMIC_RELAXED, __HIP_MEMORY_SCOPE_AGENT) == tag); }
DI void wait_flags(const gu64* f, unsigned long long tag) {
  for (unsigned spins = 0; !flags_ready(f, tag) && spins < SPIN_CAP; ++spins) __builtin_amdgcn_s_sleep(1);
}
DI void dma16_agent(srd_t srd, unsigned voff, unsigned soff, unsigned ldsaddr) {
  asm volatile("s_mov_b32 m0, %0\n\ts_nop 0\n\tbuffer_load_dwordx4 %1, %2, %3 offen sc1 lds" ::"s"(ldsaddr), "v"(voff), "s"(srd), "s"(soff) : "memory");
}

DI void oproj(char* lds, const int w, const int lane, const char* wo, char* ows, char* out, const int branch, const int j, const int idx,
              gu64* flags, const unsigned long long tag) {
  const int rb = idx / 3, cb = idx % 3;
  const int g = lane >> 4, l15 = lane & 15;
  const unsigned ldsb = (unsigned)(size_t)lds, tsc = ldsb + P2_TOUCH_LDS;
  const srd_t srdW = make_srd(wo + (size_t)j * 1024 * (DM * 2) + cb * 384, 1023 * (DM * 2) + 384);
  const srd_t srdO = make_srd(ows + WS_O + ((size_t)(branch * 80 + rb * 8) * 128 + 32 * j) * 1024, (7 * 128 + 32) * 1024);
  const unsigned r8 = (lane >> 2) & 7;
  const unsigned vW = r8 * (DM * 2) + (lane >> 5) * 64 + 16 * ((lane & 3) ^ (2 * (r8 >> 2))), vO = lane * 16;
  auto dma_w = [&](int s, int i) { dma16<0>(srdW, vW, (s * 64 + 8 * w) * (DM * 2) + i * 128, ldsb + (s % 3) * P2_STB + (w * 3 + i) * 1024); };
  auto dma_o = [&](int s, int i) { dma16_agent(srdO, vO, (w * 128 + 2 * s + i) * 1024, ldsb + (s % 3) * P2_STB + P2_O + (w * 2 + i) * 1024); };
  static_for<0, 3>([&](auto i) { dma_w(0, i); });
  static_for<0, 3>([&](auto i) { dma_w(1, i); });

  if (w == 0) {
    const int blo = (rb * 128) / SQ, bhi = (rb * 128 + 127) / SQ;
    wait_flags(flags + (branch * NB + ((lane & 4) ? bhi : blo)) * 16 + j * 4 + (lane & 3), tag);
  }
  asm volatile("s_barrier" ::: "memory");
  if (P2_BURST) {
    const char* ob = ows + WS_O + ((size_t)(branch * 80 + rb * 8) * 128 + 32 * j) * 1024;
    const unsigned l0 = lane + 64 * cb;
    touch<2>(ob + (size_t)w * 131072 + l0 * 128, tsc);
    if (lane < 22) { const unsigned l1 = 192 + 22 * cb + lane; touch<2>(ob + (size_t)w * 131072 + (l1 < 256 ? l1 : 255) * 128, tsc); }
  }
  static_for<0, 2>([&](auto i) { dma_o(0, i); });
  static_for<0, 2>([&](auto i) { dma_o(1, i); });

  const int wm = w & 1, wn = w >> 1;
  const unsigned aWb = (g >> 1) * 3072 + (4 * (g & 1) + ((lane >> 2) & 3)) * 64 + 16 * (((lane & 3) >> 1) ^ (2 * (g & 1))) + 8 * (lane & 1);
  unsigned aW[3];
#pragma unroll
  for (int n = 0; n < 3; ++n) { const unsigned nt = 3 * wn + n; aW[n] = (aWb ^ ((nt & 1) * 32)) + (nt >> 1) * 512; }
  const unsigned aO = P2_O + wm * 8192 + lane * 16;
  f32x4 acc[4][3];
  constexpr int NS = 16;
  bf16x8 od[4], wd[3];
  if (w >= 4) __builtin_amdgcn_s_setprio(1);
  static_for<0, NS>([&](auto sc) {
    constexpr int s = sc;
    if constexpr (s == 0) wait_barrier<2>(); else if constexpr (s + 1 < NS) wait_barrier<5>(); else wait_barrier<0>();
    const char* st = lds + (s % 3) * P2_STB;
    bf16x8 of[4], wf[3];
    auto rdw = [&](int kk, int n) { const unsigned a = aW[n] + kk * 4 * 3072; return lds_read_tr2(st, a, a + 2 * 3072); };
    auto rdo = [&](int kk, int m) { return lds_read128(st, aO + (m * 2 + kk) * 1024); };
    auto loads = [&]() {
      if constexpr (s + 2 < NS) {
        static_for<0, 3>([&](auto i) { dma_w(s + 2, i); });
        static_for<0, 2>([&](auto i) { dma_o(s + 2, i); });
      }
    };
    static_for<0, 3>([&](auto n) { wf[n] = rdw(0, n); });
    static_for<0, 4>([&](auto m) { of[m] = rdo(0, m); });
    SB();
    if (w < 4) loads();
    SB();
    if constexpr (s > 0) {
      static_for<0, 4>([&](auto m) { static_for<0, 3>([&](auto n) { mfma_acc(acc[m][n], wd[n], od[m]); }); SB(); });
    }
    static_for<0, 4>([&](auto mc) {
      constexpr int m = mc;
      static_for<0, 3>([&](auto n) { mfma_az<s == 0>(acc[m][n], wf[n], of[m]); });
      if constexpr (m == 0) static_for<0, 3>([&](auto n) { wd[n] = rdw(1, n); });
      if constexpr (m == 1) static_for<0, 4>([&](auto mm) { od[mm] = rdo(1, mm); });
      SB();
    });
    if (w >= 4) loads();
    SB();
  });
  static_for<0, 4>([&](auto m) { static_for<0, 3>([&](auto n) { mfma_acc(acc[m][n], wd[n], od[m]); }); });
  mfma_settle();

  char* pbase = ows + WS_PART + (size_t)((branch * 10 + rb) * 3 + cb) * (16 * 12288);
  {
    const srd_t srdP = make_srd(pbase + (size_t)j * (4 * 12288), 4 * 12288);
#pragma unroll
    for (int h = 0; h < 2; ++h) {
      const int q = 2 * wm + h;
      if (q != j) {
#pragma unroll
        for (int n = 0; n < 3; ++n) {
          const f32x4 a0 = acc[2 * h][n], a1 = acc[2 * h + 1][n];
          const u32x4 v = {cvtpk(a0[0], a0[1]), cvtpk(a0[2], a0[3]), cvtpk(a1[0], a1[1]), cvtpk(a1[2], a1[3])};
          __builtin_amdgcn_raw_buffer_store_b128(v, srdP, lane * 16 + q * 12288 + (3 * wn + n) * 1024, 0, 16);
        }
      }
    }
  }
  gu64* flags2 = flags + 512 + ((branch * 10 + rb) * 3 + cb) * 4;
  asm volatile("s_waitcnt vmcnt(0)\n\ts_barrier" ::: "memory");
  if (w == 0 && lane == 0) __hip_atomic_store(flags2 + j, tag, __ATOMIC_RELAXED, __HIP_MEMORY_SCOPE_AGENT);
  if (wm == (j >> 1)) {
    wait_flags(flags2 + ((lane & 3) == j ? (j ^ 1) : (lane & 3)), tag);
    const srd_t srdP = make_srd(pbase, 16 * 12288);
    u32x4 pv[3][3];
#pragma unroll
    for (int q = 0; q < 3; ++q) {
      const int src = (j + 1 + q) & 3;
#pragma unroll
      for (int n = 0; n < 3; ++n) pv[q][n] = __builtin_amdgcn_raw_buffer_load_b128(srdP, lane * 16 + (3 * wn + n) * 1024, (src * 4 + j) * 12288, 16);
    }
#pragma unroll
    for (int i = 0; i < 6; ++i) {
      const int mm = i / 3, n = i % 3;
      f32x4 a = (j & 1) ? acc[2 + mm][n] : acc[mm][n];
#pragma unroll
      for (int q = 0; q < 3; ++q) {
        a[0] += __uint_as_float(pv[q][n][2 * mm] << 16);
        a[1] += __uint_as_float(pv[q][n][2 * mm] & 0xFFFF0000u);
        a[2] += __uint_as_float(pv[q][n][2 * mm + 1] << 16);
        a[3] += __uint_as_float(pv[q][n][2 * mm + 1] & 0xFFFF0000u);
      }
      const int row = rb * 128 + 32 * j + 16 * mm + l15;
      const u32x2 o = {cvtpk(a[0], a[1]), cvtpk(a[2], a[3])};
      *reinterpret_cast<u32x2*>(out + (size_t)row * (2 * DM * 2) + (branch * DM + cb * 192 + (3 * wn + n) * 16 + 4 * g) * 2) = o;
    }
  }
}

__global__ __launch_bounds__(512, 2) void fused_kernel(const char* __restrict__ xA, const char* __restrict__ kA, const char* __restrict__ vA,
                                                        const char* __restrict__ wqA, const char* __restrict__ woA,
                                                        const char* __restrict__ xB, const char* __restrict__ kB, const char* __restrict__ vB,
                                                        const char* __restrict__ wqB, const char* __restrict__ woB,
                                                        char* __restrict__ out, char* __restrict__ ows) {
  extern __shared__ __attribute__((aligned(16))) char lds[];
  const int w = (int)rfl(threadIdx.x >> 6);
  const int xcd = blockIdx.x & 7, idx = blockIdx.x >> 3;
  const int branch = xcd >> 2, j = xcd & 3;
  gu64* flags = (gu64*)(ows + WS_FLAGS);
  gu64* epoch = (gu64*)(ows + WS_EPOCH);
  unsigned long long tag;
  {
    const int b = idx >> 2, head0 = 8 * j + 2 * (idx & 3);
    const char* xb = (branch ? xB : xA) + (size_t)b * SQ * DM * 2;
    const char* wqb = (branch ? wqB : wqA) + head0 * DH * 2;
    const size_t kvo = (size_t)(b * NH + head0) * SK * DH * 2;
    const char* k = (branch ? kB : kA) + kvo;
    const char* v = (branch ? vB : vA) + kvo;
    const int rowtile0 = (branch * NB + b) * 10;
    const char* wqt = (branch ? wqB : wqA) + (size_t)idx * 18 * HD * 2 + j * 2048;
    const char* xt = (branch ? xB : xA) + (size_t)idx * 46080;
    if (w < 4) attn_wave<3>(lds, w, (int)lane_id(), xb, wqb, k, v, ows, rowtile0, head0, wqt, xt);
    else attn_wave<2>(lds, w, (int)lane_id(), xb, wqb, k, v, ows, rowtile0, head0, wqt, xt);
    {
      const unsigned long long e = __hip_atomic_load(epoch, __ATOMIC_RELAXED, __HIP_MEMORY_SCOPE_AGENT) + TAG_STEP;
      tag = ((unsigned long long)rfl((unsigned)(e >> 32)) << 32) | rfl((unsigned)e);
    }
    asm volatile("s_waitcnt vmcnt(0) lgkmcnt(0)\n\ts_barrier" ::: "memory");
    if (w == 0 && lane_id() == 0) __hip_atomic_store(flags + (branch * NB + b) * 16 + j * 4 + (idx & 3), tag, __ATOMIC_RELAXED, __HIP_MEMORY_SCOPE_AGENT);
  }
  const int lane = (int)lane_id();
  const char* wo = branch ? woB : woA;
  if (P2_BURST) {
    const char* p = wo + (size_t)j * 1024 * (DM * 2) + (size_t)(idx * 288 + w * 36) * 128;
    if (lane < 36) touch<0>(p + lane * 128, (unsigned)(size_t)lds + P2_TOUCH_LDS);
  }
  if (idx < 30) {
    oproj(lds, w, lane, wo, ows, out, branch, j, idx, flags, tag);
  } else if (blockIdx.x == 255 && w == 0) {
    bool ok = false;
    for (unsigned spins = 0; !ok && spins < SPIN_CAP; ++spins) {
      ok = flags_ready(flags + lane, tag) && flags_ready(flags + 64 + lane, tag) && flags_ready(flags + 128 + lane, tag) && flags_ready(flags + 192 + lane, tag);
      if (!ok) __builtin_amdgcn_s_sleep(8);
    }
    if (lane == 0) __hip_atomic_store(epoch, tag, __ATOMIC_RELAXED, __HIP_MEMORY_SCOPE_AGENT);
  }
}

extern "C" void kernel_launch(void* const* d_in, const int* in_sizes, int n_in, void* d_out, int out_size, void* d_ws, size_t ws_size,
                              hipStream_t stream) {
  (void)in_sizes; (void)n_in; (void)out_size; (void)ws_size;
  (void)hipFuncSetAttribute((const void*)fused_kernel, hipFuncAttributeMaxDynamicSharedMemorySize, LDS_BYTES);
  fused_kernel<<<256, 512, LDS_BYTES, stream>>>((const char*)d_in[0], (const char*)d_in[1], (const char*)d_in[2], (const char*)d_in[3],
                                                (const char*)d_in[4], (const char*)d_in[5], (const char*)d_in[6], (const char*)d_in[7],
                                                (const char*)d_in[8], (const char*)d_in[9], (char*)d_out, (char*)d_ws);
}
```

```cpp
#include <hip/hip_runtime.h>
#include <hip/hip_bf16.h>

typedef short s16x4 __attribute__((ext_vector_type(4)));
typedef short s16x8 __attribute__((ext_vector_type(8)));
typedef __bf16 bf16x2 __attribute__((ext_vector_type(2)));
typedef __bf16 bf16x8 __attribute__((ext_vector_type(8)));
typedef float f32x2 __attribute__((ext_vector_type(2)));
typedef float f32x4 __attribute__((ext_vector_type(4)));
typedef unsigned u32x2 __attribute__((ext_vector_type(2)));
typedef unsigned u32x4 __attribute__((ext_vector_type(4)));
typedef __attribute__((address_space(3))) s16x4* lds_s16x4_ptr;
typedef __attribute__((address_space(1))) unsigned long long gu64;
typedef __amdgpu_buffer_rsrc_t srd_t;

#define DI __device__ __forceinline__
#define SB() __builtin_amdgcn_sched_barrier(0)
#define USE_PRIO 1
#define PRIO(x) do { if (USE_PRIO) __builtin_amdgcn_s_setprio(x); } while (0)

constexpr int NB = 8, SQ = 160, DM = 576, NH = 32, SK = 512, DH = 128;
constexpr int HD = NH * DH;
constexpr float SCALE_LOG2E = 0.08838834764831845f * 1.4426950408889634f;
constexpr float RESCALE_THR = 8.0f;
constexpr unsigned long long TAG_STEP = 0x9E3779B97F4A7C15ull;
constexpr unsigned SPIN_CAP = 1u << 20;

constexpr size_t WS_O = 0;
constexpr size_t WS_PART = (size_t)160 * 128 * 1024;
constexpr size_t WS_FLAGS = WS_PART + (size_t)60 * 16 * 12288;
constexpr size_t WS_EPOCH = WS_FLAGS + 8192;

#define KV_NT 1
#define KPD_BIG 3
#define QPD_Q 4
#define VTAIL 2
#define KPD_SMALL 4
#define VPD_BIG 3
#define VPD_SMALL 4
#define OUT_AUX 0
#define P2_BURST_W 1
#define P2_BURST_O 0
#define TOUCH_Q 0
#define TOUCH_NT 0
#define Q_NT 0

DI unsigned rfl(unsigned x) { return __builtin_amdgcn_readfirstlane(x); }
DI srd_t make_srd(const void* p, unsigned bytes) { return __builtin_amdgcn_make_buffer_rsrc((void*)p, (short)0, (int)bytes, 0x00020000); }

template <int NT>
DI void dma16(srd_t srd, unsigned voff, unsigned soff, unsigned ldsaddr) {
  if constexpr (NT)
    asm volatile("s_mov_b32 m0, %0\n\ts_nop 0\n\tbuffer_load_dwordx4 %1, %2, %3 offen nt lds" ::"s"(ldsaddr), "v"(voff), "s"(srd), "s"(soff) : "memory");
  else
    asm volatile("s_mov_b32 m0, %0\n\ts_nop 0\n\tbuffer_load_dwordx4 %1, %2, %3 offen lds" ::"s"(ldsaddr), "v"(voff), "s"(srd), "s"(soff) : "memory");
}
template <int POL>
DI void touch(const char* p, unsigned ldsaddr) {
  if constexpr (POL == 1) asm volatile("s_mov_b32 m0, %0\n\ts_nop 0\n\tglobal_load_lds_dword %1, off nt" ::"s"(ldsaddr), "v"(p) : "memory");
  else if constexpr (POL == 2) asm volatile("s_mov_b32 m0, %0\n\ts_nop 0\n\tglobal_load_lds_dword %1, off sc1" ::"s"(ldsaddr), "v"(p) : "memory");
  else asm volatile("s_mov_b32 m0, %0\n\ts_nop 0\n\tglobal_load_lds_dword %1, off" ::"s"(ldsaddr), "v"(p) : "memory");
}
template <int N>
DI void wait_barrier() { asm volatile("s_waitcnt vmcnt(%0) lgkmcnt(0)\n\ts_barrier" ::"i"(N) : "memory"); }

DI unsigned cvtpk(float lo, float hi) {
  f32x2 v = {lo, hi};
  bf16x2 b = __builtin_convertvector(v, bf16x2);
  return __builtin_bit_cast(unsigned, b);
}
DI void mfma_acc(f32x4& c, bf16x8 a, bf16x8 b) { asm volatile("v_mfma_f32_16x16x32_bf16 %0, %1, %2, %0" : "+v"(c) : "v"(a), "v"(b)); }
DI void mfma_new(f32x4& d, bf16x8 a, bf16x8 b, f32x4 c) { asm volatile("v_mfma_f32_16x16x32_bf16 %0, %1, %2, %3" : "=&v"(d) : "v"(a), "v"(b), "v"(c)); }
DI void mfma_zero(f32x4& d, bf16x8 a, bf16x8 b) { asm volatile("v_mfma_f32_16x16x32_bf16 %0, %1, %2, 0" : "=&v"(d) : "v"(a), "v"(b)); }
template <bool ZERO>
DI void mfma_az(f32x4& c, bf16x8 a, bf16x8 b) { if constexpr (ZERO) mfma_zero(c, a, b); else mfma_acc(c, a, b); }
DI void mfma_settle() { asm volatile("s_nop 7\n\ts_nop 1" ::: "memory"); __builtin_amdgcn_sched_barrier(0); }
typedef __attribute__((address_space(3))) const bf16x8* lds_bf16x8_ptr;
DI bf16x8 lds_read128(unsigned addr) { return *(lds_bf16x8_ptr)(size_t)addr; }
DI s16x4 lds_read_tr(unsigned addr) { return __builtin_amdgcn_ds_read_tr16_b64_v4i16((lds_s16x4_ptr)(size_t)addr); }
DI bf16x8 join8(s16x4 a, s16x4 b) { const s16x8 r = __builtin_shufflevector(a, b, 0, 1, 2, 3, 4, 5, 6, 7); return __builtin_bit_cast(bf16x8, r); }
DI bf16x8 lds_read_tr2(unsigned a0, unsigned a1) { return join8(lds_read_tr(a0), lds_read_tr(a1)); }
DI f32x2 splat2(float v) { f32x2 p = {v, v}; asm volatile("" : "+v"(p)); return p; }
DI f32x4 mul4(f32x4 a, f32x2 p) {
  f32x2 lo = f32x2{a[0], a[1]} * p, hi = f32x2{a[2], a[3]} * p;
  return f32x4{lo[0], lo[1], hi[0], hi[1]};
}
DI f32x4 sub4(f32x4 a, f32x2 p) {
  f32x2 lo = f32x2{a[0], a[1]} - p, hi = f32x2{a[2], a[3]} - p;
  return f32x4{lo[0], lo[1], hi[0], hi[1]};
}
DI void mfma_move(f32x4& dst, f32x4 src, bf16x8 zero) { asm volatile("s_nop 1\n\tv_mfma_f32_16x16x32_bf16 %0, %1, %1, %2" : "+v"(dst) : "v"(zero), "v"(src)); }
DI bf16x8 zero_frag() { u32x4 z = {0u, 0u, 0u, 0u}; asm volatile("" : "+v"(z)); return __builtin_bit_cast(bf16x8, z); }
DI float max3_s(float a, float b, float c) { float r; asm volatile("v_max3_f32 %0, %1, %2, %3" : "=v"(r) : "v"(a), "v"(b), "v"(c)); return r; }
DI float xmax16_32(float v) {
  unsigned u = __float_as_uint(v);
  auto a = __builtin_amdgcn_permlane16_swap(u, u, false, false);
  v = fmaxf(__uint_as_float(a[0]), __uint_as_float(a[1]));
  u = __float_as_uint(v);
  auto b = __builtin_amdgcn_permlane32_swap(u, u, false, false);
  return fmaxf(__uint_as_float(b[0]), __uint_as_float(b[1]));
}
DI unsigned lane_id() { unsigned l; asm volatile("v_mbcnt_lo_u32_b32 %0, -1, 0\n\tv_mbcnt_hi_u32_b32 %0, -1, %0" : "=v"(l)); return l; }
template <int I, int N, class F>
DI void static_for(F&& f) {
  if constexpr (I < N) { f(std::integral_constant<int, I>{}); static_for<I + 1, N>(f); }
}

constexpr unsigned KRING = 0, VRING = 65536, SLOT = 16384, QST_X = 16384;
constexpr unsigned qstage(int i) { return i == 0 ? 32768u : (i == 1 ? 98304u : 124928u); }
constexpr unsigned TOUCH_LDS = 157696;
constexpr int LDS_BYTES = 163840;

template <int NQ>
DI void attn_wave(char* lds, const int w, const int lane, const char* xb, const char* wqb, const char* kb0, const char* vb0,
                  char* ows, const int rowtile0, const int head0, const char* wq_touch, const char* x_touch) {
  constexpr bool BIG = (NQ == 3);
  const int hh = (w >> 1) & 1, wi = w & 1, j4 = w & 3;
  const int g = lane >> 4, l15 = lane & 15;
  const int qt0 = BIG ? 3 * wi : 6 + 2 * wi;
  const unsigned ldsb = (unsigned)(size_t)lds;

  const srd_t srdK0 = make_srd(kb0, SK * DH * 2), srdK1 = make_srd(kb0 + SK * DH * 2, SK * DH * 2);
  const srd_t srdV0 = make_srd(vb0, SK * DH * 2), srdV1 = make_srd(vb0 + SK * DH * 2, SK * DH * 2);
  const unsigned krow = 4 * j4 + g;
  const unsigned vK = krow * 256 + 16 * (l15 ^ krow);
  const unsigned tr_row = (lane >> 2) & 7;
  const unsigned vV = tr_row * 256 + (lane >> 5) * 64 + 16 * ((lane & 3) ^ (2 * (tr_row >> 2)));
  auto dma_k = [&](int step, int slot, int i, int head) {
    dma16<KV_NT>(head ? srdK1 : srdK0, vK, step * 8192 + i * 4096, ldsb + KRING + slot * SLOT + head * 8192 + (j4 + 4 * i) * 1024);
  };
  auto dma_v = [&](int step, int slot, int i, int head) {
    dma16<KV_NT>(head ? srdV1 : srdV0, vV, step * 8192 + j4 * 2048 + i * 128, ldsb + VRING + slot * SLOT + head * 8192 + (2 * j4 + i) * 1024);
  };
  auto issue_k = [&](int step) { dma_k(step, step & 3, 0, 0); dma_k(step, step & 3, 0, 1); dma_k(step, step & 3, 1, 0); dma_k(step, step & 3, 1, 1); };
  auto issue_v = [&](int step) { dma_v(step, step & 3, 0, 0); dma_v(step, step & 3, 0, 1); dma_v(step, step & 3, 1, 0); dma_v(step, step & 3, 1, 1); };

  const srd_t srdX = make_srd(xb, SQ * DM * 2), srdW = make_srd(wqb, (DM - 1) * HD * 2 + 512);
  const unsigned vX = (lane >> 2) * (DM * 2) + 16 * ((lane & 3) ^ ((0u - (lane >> 4)) & 3));
  const unsigned vWp = (tr_row + 8 * j4) * (HD * 2) + (lane >> 5) * 64 + 16 * ((lane & 3) ^ (2 * (j4 & 1)));
  const unsigned kbw = w >> 1;
  const unsigned vW = (tr_row + 8 * kbw) * (HD * 2) + (lane >> 5) * 64 + 16 * ((lane & 3) ^ (2 * (kbw & 1))) + (w & 1) * 256;
  const unsigned xt2 = rfl(j4 < 2 ? j4 + 8 : 9);
  const unsigned xso2 = rfl(xt2 * 16 * (DM * 2));
  auto dma_q0 = [&](int ks, int i) {
    const unsigned lb = ldsb + qstage(ks % 3);
    if (i < 4) dma16<Q_NT>(srdW, vWp, ks * 32 * (HD * 2) + i * 128, lb + (j4 * 4 + i) * 1024);
    else if (i < 6) dma16<Q_NT>(srdX, vX, (j4 + 4 * (i - 4)) * 16 * (DM * 2) + ks * 64, lb + QST_X + (j4 + 4 * (i - 4)) * 1024);
    else dma16<Q_NT>(srdX, vX, xso2 + ks * 64, lb + QST_X + xt2 * 1024);
  };
  auto dma_q = [&](int ks, int buf, int i) {
    const unsigned lb = ldsb + qstage(buf);
    if (i < 2) dma16<Q_NT>(srdW, vW, ks * 32 * (HD * 2) + i * 128, lb + (kbw * 4 + 2 * (w & 1) + i) * 1024);
    else if (i == 2) dma16<Q_NT>(srdX, vX, w * 16 * (DM * 2) + ks * 64, lb + QST_X + w * 1024);
    else if (!BIG && j4 < 2) dma16<Q_NT>(srdX, vX, xso2 + ks * 64, lb + QST_X + xt2 * 1024);
  };
  constexpr int NKS = DM / 32;
  if constexpr (!BIG) {
    static_for<0, 7>([&](auto i) { dma_q0(0, i); });
    static_for<0, 7>([&](auto i) { dma_q0(1, i); });
  } else if constexpr (TOUCH_Q) {
    const unsigned l0 = j4 * 72 + lane, l1 = j4 * 72 + 64 + (lane & 7), sc = ldsb + TOUCH_LDS;
    touch<TOUCH_NT>(wq_touch + (l0 >> 4) * (HD * 2) + (l0 & 15) * 128, sc);
    touch<TOUCH_NT>(x_touch + (j4 * 90 + lane) * 128, sc);
    touch<TOUCH_NT>(lane < 8 ? wq_touch + (l1 >> 4) * (HD * 2) + (l1 & 15) * 128 : x_touch + (j4 * 90 + 64 + (lane < 34 ? lane - 8 : 0)) * 128, sc);
  }

  auto prefetch_kv = [&](int tile) {
    if constexpr (BIG) { dma_k(tile, tile, 0, 0); dma_k(tile, tile, 0, 1); dma_k(tile, tile, 1, 0); dma_k(tile, tile, 1, 1); }
    else { dma_v(tile, tile, 0, 0); dma_v(tile, tile, 0, 1); dma_v(tile, tile, 1, 0); dma_v(tile, tile, 1, 1); }
  };
  f32x4 qa[8][NQ];
  {
    const unsigned aW0 = g * 4096 + hh * 2048 + ((lane >> 2) & 3) * 64 + 16 * (((lane & 3) >> 1) ^ (2 * (g & 1))) + 8 * (lane & 1);
    const unsigned aW1 = aW0 ^ 32;
    const unsigned aX = QST_X + qt0 * 1024 + l15 * 64 + 16 * (g ^ ((0u - (l15 >> 2)) & 3));
    unsigned bW0[3], bW1[3], bX[3];
#pragma unroll
    for (int i = 0; i < 3; ++i) {
      bW0[i] = ldsb + aW0 + qstage(i); bW1[i] = ldsb + aW1 + qstage(i); bX[i] = ldsb + aX + qstage(i);
      asm volatile("" : "+v"(bW0[i]), "+v"(bW1[i]), "+v"(bX[i]));
    }
    bf16x8 xo[NQ], wd[3];
#pragma unroll
    for (int i = 0; i < 3; ++i) wd[i] = zero_frag();
#pragma unroll
    for (int t = 0; t < NQ; ++t) xo[t] = zero_frag();
#pragma unroll
    for (int dt = 0; dt < 8; ++dt)
#pragma unroll
      for (int t = 0; t < NQ; ++t) { qa[dt][t] = f32x4{0.f, 0.f, 0.f, 0.f}; asm volatile("" : "+v"(qa[dt][t])); }
    auto qstep = [&](const int ks, auto bufc) {
      constexpr int BUF = bufc;
      if constexpr (BIG) { if (ks < 2) wait_barrier<63>(); else if (ks + 1 == NKS) wait_barrier<4>(); else wait_barrier<3>(); }
      else { if (ks == 0) wait_barrier<7>(); else if (j4 < 2 || ks + 1 == NKS) wait_barrier<4>(); else wait_barrier<3>(); }
      auto rdw_half = [&](int dt, int h) { return lds_read_tr(((dt & 1) ? bW1[BUF] : bW0[BUF]) + (dt >> 1) * 512 + h * 256); };
      bf16x8 xn[NQ], wf[8];
      s16x4 hq;
      const bool more = ks + 2 < NKS;
      constexpr int QPD = QPD_Q;
      if constexpr (!BIG) {
        if (more) { static_for<0, 4>([&](auto i) { dma_q(ks + 2, (BUF + 2) % 3, i); }); }
        else prefetch_kv(ks - (NKS - 2));
        SB();
      }
      static_for<0, 3 * NQ>([&](auto ic) {
        constexpr int i = ic, d = i / NQ, t = i % NQ;
        mfma_acc(qa[5 + d][t], wd[d], xo[t]);
        SB();
        constexpr int NX = NQ, NR = 2 * QPD + NX;
        static_for<(i * NR) / (3 * NQ), ((i + 1) * NR) / (3 * NQ)>([&](auto rc) {
          constexpr int r = rc;
          if constexpr (r < 2) { if constexpr (r == 0) hq = rdw_half(0, 0); else wf[0] = join8(hq, rdw_half(0, 1)); }
          else if constexpr (r < 2 + NX) xn[r - 2] = lds_read128(bX[BUF] + (r - 2) * 1024);
          else { constexpr int j = r - 2 - NX, dt = 1 + j / 2; if constexpr ((j & 1) == 0) hq = rdw_half(dt, 0); else wf[dt] = join8(hq, rdw_half(dt, 1)); }
        });
        SB();
      });
      static_for<0, 5>([&](auto dtc) {
        constexpr int dt = dtc;
        s16x4 h0, h1;
        static_for<0, NQ>([&](auto tc) {
          constexpr int t = tc;
          mfma_acc(qa[dt][t], wf[dt], xn[t]);
          SB();
          if constexpr (dt + QPD < 8) {
            if constexpr (t == 0) h0 = rdw_half(dt + QPD, 0);
            if constexpr (t == 1) { h1 = rdw_half(dt + QPD, 1); wf[dt + QPD] = join8(h0, h1); }
          }
          SB();
        });
      });
      if constexpr (BIG) {
        if (more) { static_for<0, 3>([&](auto i) { dma_q(ks + 2, (BUF + 2) % 3, i); }); }
        else prefetch_kv(ks - (NKS - 2));
        SB();
      }
#pragma unroll
      for (int i = 0; i < 3; ++i) wd[i] = wf[5 + i];
#pragma unroll
      for (int t = 0; t < NQ; ++t) xo[t] = xn[t];
    };
#pragma unroll 1
    for (int ks = 0; ks < NKS; ks += 3) {
      qstep(ks, std::integral_constant<int, 0>{});
      qstep(ks + 1, std::integral_constant<int, 1>{});
      qstep(ks + 2, std::integral_constant<int, 2>{});
    }
#pragma unroll
    for (int i = 0; i < 3; ++i)
#pragma unroll
      for (int t = 0; t < NQ; ++t) mfma_acc(qa[5 + i][t], wd[i], xo[t]);
  }
  mfma_settle();
  if constexpr (BIG) wait_barrier<4>(); else wait_barrier<63>();
  bf16x8 qf[NQ][4];
  const f32x2 cc = splat2(SCALE_LOG2E);
#pragma unroll
  for (int t = 0; t < NQ; ++t)
#pragma unroll
    for (int s = 0; s < 4; ++s) {
      const f32x4 q0 = mul4(qa[2 * s][t], cc), q1 = mul4(qa[2 * s + 1][t], cc);
      unsigned r0a = cvtpk(q0[0], q0[1]), r0b = cvtpk(q0[2], q0[3]), r1a = cvtpk(q1[0], q1[1]), r1b = cvtpk(q1[2], q1[3]);
      auto a32 = __builtin_amdgcn_permlane32_swap(r0a, r1a, false, false);
      auto b32 = __builtin_amdgcn_permlane32_swap(r0b, r1b, false, false);
      auto a16 = __builtin_amdgcn_permlane16_swap(a32[0], a32[1], false, false);
      auto b16 = __builtin_amdgcn_permlane16_swap(b32[0], b32[1], false, false);
      u32x4 f = {a16[0], b16[0], a16[1], b16[1]};
      qf[t][s] = __builtin_bit_cast(bf16x8, f);
    }
  if constexpr (!BIG) issue_k(2);

  f32x4 oa[8][NQ], la[NQ], nm[NQ], sa[2][NQ];
#pragma unroll
  for (int t = 0; t < NQ; ++t) {
    la[t] = f32x4{0.f, 0.f, 0.f, 0.f};
    asm volatile("" : "+v"(la[t]));
#pragma unroll
    for (int dt = 0; dt < 8; ++dt) { oa[dt][t] = f32x4{0.f, 0.f, 0.f, 0.f}; asm volatile("" : "+v"(oa[dt][t])); }
  }
  const u32x4 onesu = {0x3F803F80u, 0x3F803F80u, 0x3F803F80u, 0x3F803F80u};
  bf16x8 ones = __builtin_bit_cast(bf16x8, onesu);
  asm volatile("" : "+v"(ones));
  unsigned aK[4];
#pragma unroll
  for (int s = 0; s < 4; ++s) { aK[s] = ldsb + KRING + hh * 8192 + l15 * 256 + 16 * ((4 * s + g) ^ l15); asm volatile("" : "+v"(aK[s])); }
  unsigned aV0 = ldsb + VRING + hh * 8192 + (g >> 1) * 2048 + (4 * (g & 1) + ((lane >> 2) & 3)) * 64 + 16 * (((lane & 3) >> 1) ^ (2 * (g & 1))) + 8 * (lane & 1);
  unsigned aV1 = aV0 ^ 32;
  asm volatile("" : "+v"(aV0), "+v"(aV1));
  auto rdk = [&](int slot, int i) { return lds_read128(aK[i & 3] + slot * SLOT + (i >> 2) * 4096); };
  auto rdv = [&](int slot, int dt) { const unsigned a = ((dt & 1) ? aV1 : aV0) + slot * SLOT + (dt >> 1) * 512; return lds_read_tr2(a, a + 4096); };
  bf16x8 pf[NQ];
  constexpr int NST = SK / 32;
  constexpr int KPD = BIG ? KPD_BIG : KPD_SMALL, VPD = BIG ? VPD_BIG : VPD_SMALL;

  auto rdv_half = [&](int slot, int dt, int h) { return lds_read_tr(((dt & 1) ? aV1 : aV0) + slot * SLOT + (dt >> 1) * 512 + h * 4096); };
  auto scores = [&](auto firstc, const int kslot, bf16x8 (&kf)[8], auto vpre, const int vslot, bf16x8 (&vf)[8]) {
    constexpr bool FIRST = firstc, VPRE = vpre;
    static_for<0, 8>([&](auto gc) {
      constexpr int gi = gc, kt = gi >> 2, s = gi & 3;
      constexpr bool RV = VPRE && gi >= 8 - VPD;
      s16x4 h0, h1;
      static_for<0, NQ>([&](auto tc) {
        constexpr int t = tc;
        if constexpr (s > 0) mfma_acc(sa[kt][t], kf[gi], qf[t][s]);
        else if constexpr (FIRST) mfma_zero(sa[kt][t], kf[gi], qf[t][s]);
        else mfma_new(sa[kt][t], kf[gi], qf[t][s], nm[t]);
        SB();
        if constexpr (t == 0 && gi + KPD < 8) kf[gi + KPD] = rdk(kslot, gi + KPD);
        if constexpr (RV && t == NQ - 2) h0 = rdv_half(vslot, gi - (8 - VPD), 0);
        if constexpr (RV && t == NQ - 1) { h1 = rdv_half(vslot, gi - (8 - VPD), 1); vf[gi - (8 - VPD)] = join8(h0, h1); }
        SB();
      });
    });
  };
  auto values = [&](auto d0c, auto d1c, const int vslot, bf16x8 (&vf)[8], auto kpre, const int kslot, bf16x8 (&kf)[8]) {
    constexpr int D0 = d0c, D1 = d1c;
    constexpr bool KPRE = kpre;
    if constexpr (D0 == 0) {
#pragma unroll
      for (int t = 0; t < NQ; ++t) mfma_acc(la[t], ones, pf[t]);
    }
    static_for<D0, D1>([&](auto dc) {
      constexpr int dt = dc;
      constexpr bool RV = dt + VPD < 8, RK = KPRE && dt >= 8 - KPD;
      s16x4 h0, h1;
      static_for<0, NQ>([&](auto tc) {
        constexpr int t = tc;
        mfma_acc(oa[dt][t], vf[dt], pf[t]);
        SB();
        if constexpr (RV && t == 0) h0 = rdv_half(vslot, dt + VPD, 0);
        if constexpr (RV && t == 1) { h1 = rdv_half(vslot, dt + VPD, 1); vf[dt + VPD] = join8(h0, h1); }
        if constexpr (RK && t == NQ - 1) kf[dt - (8 - KPD)] = rdk(kslot, dt - (8 - KPD));
        SB();
      });
    });
  };
  auto softmax = [&](auto firstc) {
    constexpr bool FIRST = firstc;
    float pmt[NQ];
#pragma unroll
    for (int t = 0; t < NQ; ++t) pmt[t] = max3_s(sa[0][t][0], sa[0][t][1], sa[0][t][2]);
#pragma unroll
    for (int t = 0; t < NQ; ++t) pmt[t] = max3_s(pmt[t], sa[0][t][3], sa[1][t][0]);
#pragma unroll
    for (int t = 0; t < NQ; ++t) pmt[t] = max3_s(pmt[t], sa[1][t][1], sa[1][t][2]);
    float pm = max3_s(pmt[0], sa[1][0][3], sa[1][1][3]);
    if constexpr (NQ == 3) pm = max3_s(pm, pmt[2], sa[1][2][3]);
    pm = fmaxf(pm, pmt[1]);
    if (FIRST || __builtin_expect(__any(pm > RESCALE_THR), 0)) {
#pragma unroll
      for (int t = 0; t < NQ; ++t) {
        float rm = fmaxf(fmaxf(fmaxf(sa[0][t][0], sa[0][t][1]), fmaxf(sa[0][t][2], sa[0][t][3])),
                         fmaxf(fmaxf(sa[1][t][0], sa[1][t][1]), fmaxf(sa[1][t][2], sa[1][t][3])));
        rm = xmax16_32(rm);
        const float dl = FIRST ? rm : fmaxf(rm, 0.f);
        const f32x2 dd = splat2(dl);
        if constexpr (FIRST) {
          nm[t] = sub4(f32x4{0.f, 0.f, 0.f, 0.f}, dd);
          sa[0][t] = sub4(sa[0][t], dd);
          sa[1][t] = sub4(sa[1][t], dd);
        } else {
          const bf16x8 z = zero_frag();
          const f32x2 ff = splat2(__builtin_amdgcn_exp2f(-dl));
          mfma_move(nm[t], sub4(nm[t], dd), z);
          mfma_move(sa[0][t], sub4(sa[0][t], dd), z);
          mfma_move(sa[1][t], sub4(sa[1][t], dd), z);
          mfma_move(la[t], mul4(la[t], ff), z);
#pragma unroll
          for (int dt = 0; dt < 8; ++dt) mfma_move(oa[dt][t], mul4(oa[dt][t], ff), z);
        }
      }
      if constexpr (!FIRST) mfma_settle();
    }
    SB();
    float ex[NQ][8];
#pragma unroll
    for (int t = 0; t < NQ; ++t)
#pragma unroll
      for (int e = 0; e < 8; ++e) ex[t][e] = __builtin_amdgcn_exp2f(sa[e >> 2][t][e & 3]);
#pragma unroll
    for (int t = 0; t < NQ; ++t) {
      const u32x4 u = {cvtpk(ex[t][0], ex[t][1]), cvtpk(ex[t][2], ex[t][3]), cvtpk(ex[t][4], ex[t][5]), cvtpk(ex[t][6], ex[t][7])};
      pf[t] = __builtin_bit_cast(bf16x8, u);
      asm volatile("" : "+v"(pf[t]));
    }
    asm volatile("s_nop 1" ::: "memory");
    SB();
  };

  using I0 = std::integral_constant<int, 0>; using I1 = std::integral_constant<int, 1>;
  using I2 = std::integral_constant<int, 2>; using I3 = std::integral_constant<int, 3>; using I4 = std::integral_constant<int, 4>;
  using T_ = std::true_type; using F_ = std::false_type;
  static_assert(VTAIL <= VPD_BIG, "the fragments of the tail tiles are read before the barrier that frees their slot");
  using I6 = std::integral_constant<int, 8 - VTAIL>; using I8 = std::integral_constant<int, 8>;
  bf16x8 kf[8], vf[8];
  if constexpr (BIG) {
    PRIO(1);
    static_for<0, KPD>([&](auto i) { kf[i] = rdk(0, i); });
    auto step = [&](auto slc, auto modec) {
      constexpr int SL = slc, MODE = modec;
      if constexpr (MODE == 0) scores(T_{}, SL, kf, F_{}, 0, vf);
      else {
        scores(F_{}, SL, kf, T_{}, (SL + 3) & 3, vf);
        values(I0{}, I6{}, (SL + 3) & 3, vf, F_{}, 0, kf);
      }
      wait_barrier<0>();
      if constexpr (MODE != 4) static_for<0, KPD>([&](auto i) { kf[i] = rdk((SL + 1) & 3, i); });
      if constexpr (MODE != 0) values(I6{}, I8{}, (SL + 3) & 3, vf, F_{}, 0, kf);
      SB();
      PRIO(0);
      softmax(std::integral_constant<bool, MODE == 0>{});
      PRIO(1);
    };
    step(I0{}, I0{});
#pragma unroll 1
    for (int st = 1; st < NST - 3; st += 4) { step(I1{}, I1{}); step(I2{}, I1{}); step(I3{}, I1{}); step(I0{}, I1{}); }
    step(I1{}, I1{}); step(I2{}, I1{}); step(I3{}, I4{});
    static_for<0, VPD>([&](auto i) { vf[i] = rdv(3, i); });
    values(I0{}, I8{}, 3, vf, F_{}, 0, kf);
  } else {
    static_for<0, KPD>([&](auto i) { kf[i] = rdk(0, i); });
    scores(T_{}, 0, kf, F_{}, 0, vf);
    auto step = [&](const int st, auto slc, auto modec) {
      constexpr int SL = slc, MODE = modec;
      if constexpr (MODE <= 2) { dma_v(st + 2, (SL + 2) & 3, 0, 0); dma_v(st + 2, (SL + 2) & 3, 0, 1); dma_v(st + 2, (SL + 2) & 3, 1, 0); dma_v(st + 2, (SL + 2) & 3, 1, 1); }
      if constexpr (MODE <= 1) { dma_k(st + 3, (SL + 3) & 3, 0, 0); dma_k(st + 3, (SL + 3) & 3, 0, 1); dma_k(st + 3, (SL + 3) & 3, 1, 0); dma_k(st + 3, (SL + 3) & 3, 1, 1); }
      if constexpr (MODE >= 2) asm volatile("s_nop 7" ::: "memory");
      SB();
      softmax(std::integral_constant<bool, MODE == 0>{});
      if constexpr (MODE <= 1) wait_barrier<16>(); else if constexpr (MODE == 2) wait_barrier<12>(); else if constexpr (MODE == 3) wait_barrier<4>(); else wait_barrier<0>();
      static_for<0, VPD>([&](auto i) { vf[i] = rdv(SL, i); });
      SB();
      PRIO(1);
      if constexpr (MODE != 4) {
        values(I0{}, I8{}, SL, vf, T_{}, (SL + 1) & 3, kf);
        scores(F_{}, (SL + 1) & 3, kf, F_{}, 0, vf);
      } else values(I0{}, I8{}, SL, vf, F_{}, 0, kf);
      PRIO(0);
    };
    step(0, I0{}, I0{});
#pragma unroll 1
    for (int st = 1; st < NST - 3; st += 4) { step(st, I1{}, I1{}); step(st + 1, I2{}, I1{}); step(st + 2, I3{}, I1{}); step(st + 3, I0{}, I1{}); }
    step(NST - 3, I1{}, I2{});
    step(NST - 2, I2{}, I3{});
    step(NST - 1, I3{}, I4{});
  }
  PRIO(0);
  mfma_settle();

  const unsigned lane2 = lane_id();
  const srd_t srdO = make_srd(ows + WS_O + ((size_t)(rowtile0 + qt0) * 128 + (head0 + hh) * 4) * 1024, NQ * 128 * 1024);
#pragma unroll
  for (int t = 0; t < NQ; ++t) {
    const f32x2 inv = splat2(1.0f / la[t][0]);
#pragma unroll
    for (int u = 0; u < 4; ++u) {
      const f32x4 o0 = mul4(oa[2 * u][t], inv), o1 = mul4(oa[2 * u + 1][t], inv);
      const u32x4 o = {cvtpk(o0[0], o0[1]), cvtpk(o0[2], o0[3]), cvtpk(o1[0], o1[1]), cvtpk(o1[2], o1[3])};
      __builtin_amdgcn_raw_buffer_store_b128(o, srdO, lane2 * 16 + (t * 128 + u) * 1024, 0, 16);
    }
  }
}

constexpr unsigned P2_STB = 40960, P2_O = 24576, P2_TOUCH_LDS = 3 * P2_STB;

DI bool flags_ready(const gu64* f, unsigned long long tag) { return __all(__hip_atomic_load(f, __ATOMIC_RELAXED, __HIP_MEMORY_SCOPE_AGENT) == tag); }
DI void wait_flags(const gu64* f, unsigned long long tag) {
  for (unsigned spins = 0; !flags_ready(f, tag) && spins < SPIN_CAP; ++spins) __builtin_amdgcn_s_sleep(1);
}
DI void dma16_agent(srd_t srd, unsigned voff, unsigned soff, unsigned ldsaddr) {
  asm volatile("s_mov_b32 m0, %0\n\ts_nop 0\n\tbuffer_load_dwordx4 %1, %2, %3 offen sc1 lds" ::"s"(ldsaddr), "v"(voff), "s"(srd), "s"(soff) : "memory");
}

DI void oproj(char* lds, const int w, const int lane, const char* wo, char* ows, char* out, const int branch, const int j, const int idx,
              gu64* flags, const unsigned long long tag) {
  const int rb = idx / 3, cb = idx % 3;
  const int g = lane >> 4, l15 = lane & 15;
  const unsigned ldsb = (unsigned)(size_t)lds, tsc = ldsb + P2_TOUCH_LDS;
  const srd_t srdW = make_srd(wo + (size_t)j * 1024 * (DM * 2) + cb * 384, 1023 * (DM * 2) + 384);
  const srd_t srdO = make_srd(ows + WS_O + ((size_t)(branch * 80 + rb * 8) * 128 + 32 * j) * 1024, (7 * 128 + 32) * 1024);
  const unsigned r8 = (lane >> 2) & 7;
  const unsigned vW = r8 * (DM * 2) + (lane >> 5) * 64 + 16 * ((lane & 3) ^ (2 * (r8 >> 2))), vO = lane * 16;
  auto dma_w = [&](int s, int buf, int i) { dma16<0>(srdW, vW, (s * 64 + 8 * w) * (DM * 2) + i * 128, ldsb + buf * P2_STB + (w * 3 + i) * 1024); };
  auto dma_o = [&](int s, int buf, int i) { dma16_agent(srdO, vO, (w * 128 + 2 * s + i) * 1024, ldsb + buf * P2_STB + P2_O + (w * 2 + i) * 1024); };
  static_for<0, 3>([&](auto i) { dma_w(0, 0, i); });
  static_for<0, 3>([&](auto i) { dma_w(1, 1, i); });

  if (w == 0) {
    const int blo = (rb * 128) / SQ, bhi = (rb * 128 + 127) / SQ;
    wait_flags(flags + (branch * NB + ((lane & 4) ? bhi : blo)) * 16 + j * 4 + (lane & 3), tag);
  }
  asm volatile("s_barrier" ::: "memory");
  if (P2_BURST_O) {
    const char* ob = ows + WS_O + ((size_t)(branch * 80 + rb * 8) * 128 + 32 * j) * 1024;
    const unsigned l0 = lane + 64 * cb;
    touch<2>(ob + (size_t)w * 131072 + l0 * 128, tsc);
    if (lane < 22) { const unsigned l1 = 192 + 22 * cb + lane; touch<2>(ob + (size_t)w * 131072 + (l1 < 256 ? l1 : 255) * 128, tsc); }
  }
  static_for<0, 2>([&](auto i) { dma_o(0, 0, i); });
  static_for<0, 2>([&](auto i) { dma_o(1, 1, i); });

  const int wm = w & 1, wn = w >> 1;
  const unsigned aWb = (g >> 1) * 3072 + (4 * (g & 1) + ((lane >> 2) & 3)) * 64 + 16 * (((lane & 3) >> 1) ^ (2 * (g & 1))) + 8 * (lane & 1);
  unsigned aW[3];
#pragma unroll
  for (int n = 0; n < 3; ++n) { const unsigned nt = 3 * wn + n; aW[n] = (aWb ^ ((nt & 1) * 32)) + (nt >> 1) * 512; }
  const unsigned aO = P2_O + wm * 8192 + lane * 16;
  unsigned bW[3][3], bO[3];
#pragma unroll
  for (int i = 0; i < 3; ++i) {
    bO[i] = ldsb + aO + i * P2_STB;
    asm volatile("" : "+v"(bO[i]));
#pragma unroll
    for (int n = 0; n < 3; ++n) { bW[i][n] = ldsb + aW[n] + i * P2_STB; asm volatile("" : "+v"(bW[i][n])); }
  }
  f32x4 acc[4][3];
  constexpr int NS = 16;
  bf16x8 od[4], wd[3];
  static_for<0, 4>([&](auto m) {
    od[m] = zero_frag();
    static_for<0, 3>([&](auto n) { acc[m][n] = f32x4{0.f, 0.f, 0.f, 0.f}; asm volatile("" : "+v"(acc[m][n])); });
  });
  static_for<0, 3>([&](auto n) { wd[n] = zero_frag(); });
  if (w >= 4) __builtin_amdgcn_s_setprio(1);
  auto stage = [&](const int s, auto bufc) {
    constexpr int BUF = bufc;
    if (s == 0) wait_barrier<2>(); else if (s + 1 < NS) wait_barrier<5>(); else wait_barrier<0>();
    bf16x8 of[4], wf[3];
    auto rdw_half = [&](int kk, int n, int h) { return lds_read_tr(bW[BUF][n] + kk * 4 * 3072 + h * 2 * 3072); };
    auto rdo = [&](int kk, int m) { return lds_read128(bO[BUF] + (m * 2 + kk) * 1024); };
    auto loads = [&]() {
      static_for<0, 3>([&](auto i) { dma_w(s + 2, (BUF + 2) % 3, i); });
      static_for<0, 2>([&](auto i) { dma_o(s + 2, (BUF + 2) % 3, i); });
    };
    const bool more = s + 2 < NS;
    if (w < 4 && more) loads();
    SB();
    auto reads = [&](auto ic, auto kkc, bf16x8 (&wv)[3], bf16x8 (&ov)[4], s16x4& h) {
      constexpr int i = ic, kk = kkc;
      if constexpr (i == 0 || i == 3 || i == 5) h = rdw_half(kk, i == 0 ? 0 : (i - 1) / 2, 0);
      else if constexpr (i == 1 || i == 4 || i == 6) wv[i == 1 ? 0 : (i - 2) / 2] = join8(h, rdw_half(kk, i == 1 ? 0 : (i - 2) / 2, 1));
      else if constexpr (i == 2) ov[0] = rdo(kk, 0);
      else if constexpr (i < 10) ov[i - 6] = rdo(kk, i - 6);
    };
    s16x4 hh;
    static_for<0, 12>([&](auto ic) {
      constexpr int i = ic, m = i / 3, n = i % 3;
      mfma_acc(acc[m][n], wd[n], od[m]);
      SB();
      reads(ic, std::integral_constant<int, 0>{}, wf, of, hh);
      SB();
    });
    static_for<0, 12>([&](auto ic) {
      constexpr int i = ic, m = i / 3, n = i % 3;
      mfma_acc(acc[m][n], wf[n], of[m]);
      SB();
      reads(ic, std::integral_constant<int, 1>{}, wd, od, hh);
      SB();
    });
    if (w >= 4 && more) loads();
    SB();
  };
#pragma unroll 1
  for (int s = 0; s < NS - 1; s += 3) {
    stage(s, std::integral_constant<int, 0>{});
    stage(s + 1, std::integral_constant<int, 1>{});
    stage(s + 2, std::integral_constant<int, 2>{});
  }
  stage(NS - 1, std::integral_constant<int, 0>{});
  static_for<0, 4>([&](auto m) { static_for<0, 3>([&](auto n) { mfma_acc(acc[m][n], wd[n], od[m]); }); });
  mfma_settle();

  char* pbase = ows + WS_PART + (size_t)((branch * 10 + rb) * 3 + cb) * (16 * 12288);
  {
    const srd_t srdP = make_srd(pbase + (size_t)j * (4 * 12288), 4 * 12288);
#pragma unroll
    for (int h = 0; h < 2; ++h) {
      const int q = 2 * wm + h;
      if (q != j) {
#pragma unroll
        for (int n = 0; n < 3; ++n) {
          const f32x4 a0 = acc[2 * h][n], a1 = acc[2 * h + 1][n];
          const u32x4 v = {cvtpk(a0[0], a0[1]), cvtpk(a0[2], a0[3]), cvtpk(a1[0], a1[1]), cvtpk(a1[2], a1[3])};
          __builtin_amdgcn_raw_buffer_store_b128(v, srdP, lane * 16 + q * 12288 + (3 * wn + n) * 1024, 0, 16);
        }
      }
    }
  }
  gu64* flags2 = flags + 512 + ((branch * 10 + rb) * 3 + cb) * 4;
  asm volatile("s_waitcnt vmcnt(0)\n\ts_barrier" ::: "memory");
  if (w == 0 && lane == 0) __hip_atomic_store(flags2 + j, tag, __ATOMIC_RELAXED, __HIP_MEMORY_SCOPE_AGENT);
  if (wm == (j >> 1)) {
    wait_flags(flags2 + ((lane & 3) == j ? (j ^ 1) : (lane & 3)), tag);
    const srd_t srdP = make_srd(pbase, 16 * 12288);
    const srd_t srdOut = make_srd(out + (size_t)(rb * 128 + 32 * j) * (2 * DM * 2) + (branch * DM + cb * 192) * 2, 31 * (2 * DM * 2) + 384);
    u32x4 pv[3][3];
#pragma unroll
    for (int q = 0; q < 3; ++q) {
      const int src = (j + 1 + q) & 3;
#pragma unroll
      for (int n = 0; n < 3; ++n) pv[q][n] = __builtin_amdgcn_raw_buffer_load_b128(srdP, lane * 16 + (3 * wn + n) * 1024, (src * 4 + j) * 12288, 16);
    }
#pragma unroll
    for (int i = 0; i < 6; ++i) {
      const int mm = i / 3, n = i % 3;
      f32x4 a = (j & 1) ? acc[2 + mm][n] : acc[mm][n];
#pragma unroll
      for (int q = 0; q < 3; ++q) {
        a[0] += __uint_as_float(pv[q][n][2 * mm] << 16);
        a[1] += __uint_as_float(pv[q][n][2 * mm] & 0xFFFF0000u);
        a[2] += __uint_as_float(pv[q][n][2 * mm + 1] << 16);
        a[3] += __uint_as_float(pv[q][n][2 * mm + 1] & 0xFFFF0000u);
      }
      const u32x2 o = {cvtpk(a[0], a[1]), cvtpk(a[2], a[3])};
      __builtin_amdgcn_raw_buffer_store_b64(o, srdOut, (16 * mm + l15) * (2 * DM * 2) + ((3 * wn + n) * 16 + 4 * g) * 2, 0, OUT_AUX);
    }
  }
}

__global__ __launch_bounds__(512, 2) void fused_kernel(const char* __restrict__ xA, const char* __restrict__ kA, const char* __restrict__ vA,
                                                        const char* __restrict__ wqA, const char* __restrict__ woA,
                                                        const char* __restrict__ xB, const char* __restrict__ kB, const char* __restrict__ vB,
                                                        const char* __restrict__ wqB, const char* __restrict__ woB,
                                                        char* __restrict__ out, char* __restrict__ ows) {
  extern __shared__ __attribute__((aligned(16))) char lds[];
  const int w = (int)rfl(threadIdx.x >> 6);
  const int xcd = blockIdx.x & 7, idx = blockIdx.x >> 3;
  const int branch = xcd >> 2, j = xcd & 3;
  gu64* flags = (gu64*)(ows + WS_FLAGS);
  gu64* epoch = (gu64*)(ows + WS_EPOCH);
  unsigned long long tag;
  {
    const int b = idx >> 2, head0 = 8 * j + 2 * (idx & 3);
    const char* xb = (branch ? xB : xA) + (size_t)b * SQ * DM * 2;
    const char* wqb = (branch ? wqB : wqA) + head0 * DH * 2;
    const size_t kvo = (size_t)(b * NH + head0) * SK * DH * 2;
    const char* k = (branch ? kB : kA) + kvo;
    const char* v = (branch ? vB : vA) + kvo;
    const int rowtile0 = (branch * NB + b) * 10;
    const char* wqt = (branch ? wqB : wqA) + (size_t)idx * 18 * HD * 2 + j * 2048;
    const char* xt = (branch ? xB : xA) + (size_t)idx * 46080;
    if (w < 4) attn_wave<3>(lds, w, (int)lane_id(), xb, wqb, k, v, ows, rowtile0, head0, wqt, xt);
    else attn_wave<2>(lds, w, (int)lane_id(), xb, wqb, k, v, ows, rowtile0, head0, wqt, xt);
    {
      const unsigned long long e = __hip_atomic_load(epoch, __ATOMIC_RELAXED, __HIP_MEMORY_SCOPE_AGENT) + TAG_STEP;
      tag = ((unsigned long long)rfl((unsigned)(e >> 32)) << 32) | rfl((unsigned)e);
    }
    asm volatile("s_waitcnt vmcnt(0) lgkmcnt(0)\n\ts_barrier" ::: "memory");
    if (w == 0 && lane_id() == 0) __hip_atomic_store(flags + (branch * NB + b) * 16 + j * 4 + (idx & 3), tag, __ATOMIC_RELAXED, __HIP_MEMORY_SCOPE_AGENT);
  }
  const int lane = (int)lane_id();
  const char* wo = branch ? woB : woA;
  if (P2_BURST_W) {
    const char* p = wo + (size_t)j * 1024 * (DM * 2) + (size_t)(idx * 288 + w * 36) * 128;
    if (lane < 36) touch<0>(p + lane * 128, (unsigned)(size_t)lds + P2_TOUCH_LDS);
  }
  if (idx < 30) {
    oproj(lds, w, lane, wo, ows, out, branch, j, idx, flags, tag);
  } else if (blockIdx.x == 255 && w == 0) {
    bool ok = false;
    for (unsigned spins = 0; !ok && spins < SPIN_CAP; ++spins) {
      ok = flags_ready(flags + lane, tag) && flags_ready(flags + 64 + lane, tag) && flags_ready(flags + 128 + lane, tag) && flags_ready(flags + 192 + lane, tag);
      if (!ok) __builtin_amdgcn_s_sleep(8);
    }
    if (lane == 0) __hip_atomic_store(epoch, tag, __ATOMIC_RELAXED, __HIP_MEMORY_SCOPE_AGENT);
  }
}

extern "C" void kernel_launch(void* const* d_in, const int* in_sizes, int n_in, void* d_out, int out_size, void* d_ws, size_t ws_size,
                              hipStream_t stream) {
  (void)in_sizes; (void)n_in; (void)out_size; (void)ws_size;
  (void)hipFuncSetAttribute((const void*)fused_kernel, hipFuncAttributeMaxDynamicSharedMemorySize, LDS_BYTES);
  fused_kernel<<<256, 512, LDS_BYTES, stream>>>((const char*)d_in[0], (const char*)d_in[1], (const char*)d_in[2], (const char*)d_in[3],
                                                (const char*)d_in[4], (const char*)d_in[5], (const char*)d_in[6], (const char*)d_in[7],
                                                (const char*)d_in[8], (const char*)d_in[9], (char*)d_out, (char*)d_ws);
}
```
